```python
import torch

def _softmax(x, axis=-1):
    return torch.softmax(x, dim=axis)
def _gelu(x):
    return torch.nn.functional.gelu(x, approximate="tanh")
def _silu(x):
    return torch.nn.functional.silu(x)
def _sigmoid(x):
    return torch.sigmoid(x)
def _layernorm(x, axis=-1, eps=1e-5):
    m = x.mean(dim=axis, keepdim=True)
    v = x.var(dim=axis, keepdim=True, unbiased=False)
    return (x - m) / torch.sqrt(v + eps)
def _scatter_add(tgt, idx, src, axis=0):
    return tgt.clone().index_add_(axis, idx, src)
def _segment_sum(src, seg, n):
    out = torch.zeros((n,) + src.shape[1:], dtype=src.dtype)
    return out.index_add_(0, seg, src)
def _pad(x, pw):
    flat = tuple(v for pair in reversed(pw) for v in pair)
    return torch.nn.functional.pad(x, flat)

kernel_name = 'interleave_layers__sliding_block_cached__mixer_channel_mlp__decode'

def setup_inputs():

    _w = (lambda a: (lambda t: t.to(torch.bfloat16) if t.is_floating_point() else t)(torch.as_tensor(a)))
    def _A_setup():
        B, Sq, D = 8, 1, 640
        Hq, Hkv, Sk, Dh = 32, 32, 4096, 128
        assert Hq % Hkv == 0, (Hq, Hkv)
        return {
            "x": _w((lambda *s: torch.randn(*s, dtype=torch.bfloat16) * 0.05)(B, Sq, D)),
            "k_cache": _w((lambda *s: torch.randn(*s, dtype=torch.bfloat16) * 3.2)(B, Hkv, Sk, Dh)),
            "v_cache": _w((lambda *s: torch.randn(*s, dtype=torch.bfloat16) * 0.4)(B, Hkv, Sk, Dh)),
            "wq": _w((lambda *s: torch.randn(*s, dtype=torch.bfloat16) * 3.2)(D, Hq * Dh) * (D ** -0.5)),
            "wo": _w((lambda *s: torch.randn(*s, dtype=torch.bfloat16) * 0.4)(Hq * Dh, D) * ((Hq * Dh) ** -0.5)),
        }
    _A_in = _A_setup()

    def _B_setup():
        B, S, D, F = 8, 1, 640, 1728
        return {
            "x": _w((lambda *s: torch.randn(*s, dtype=torch.bfloat16) * 0.05)(B, S, D)),
            "w1": _w((lambda *s: torch.randn(*s, dtype=torch.bfloat16) * 1.6)(D, F) * (D ** -0.5)),
            "b1": _w((lambda *s: torch.randn(*s, dtype=torch.bfloat16) * 1.6)(F)),
            "w2": _w((lambda *s: torch.randn(*s, dtype=torch.bfloat16) * 0.1)(F, D) * (F ** -0.5)),
            "b2": _w((lambda *s: torch.randn(*s, dtype=torch.bfloat16) * 0.1)(D)),
        }
    _B_in = _B_setup()

    return {
        "A_x": _A_in["x"],
        "A_k_cache": _A_in["k_cache"],
        "A_v_cache": _A_in["v_cache"],
        "A_wq": _A_in["wq"],
        "A_wo": _A_in["wo"],
        "B_x": _B_in["x"],
        "B_w1": _B_in["w1"],
        "B_b1": _B_in["b1"],
        "B_w2": _B_in["w2"],
        "B_b2": _B_in["b2"],
    }

def reference(A_x, A_k_cache, A_v_cache, A_wq, A_wo, B_x, B_w1, B_b1, B_w2, B_b2):
    def _A_ref(x, k_cache, v_cache, wq, wo):
        B, Sq, D = x.shape
        Hq, Dh = 32, 128
        Hkv, Sk = k_cache.shape[1], k_cache.shape[2]
        W = min(4096, max(256, Sk // 4))
        rep = Hq // Hkv
        q = (lambda x, *d: x.permute(*d))((x @ wq).reshape(B, Sq, Hq, Dh), 0, 2, 1, 3)
        k = (lambda x, n, axis: torch.repeat_interleave(x, n, dim=axis))(k_cache[:, :, Sk - W:, :], rep, axis=1)
        v = (lambda x, n, axis: torch.repeat_interleave(x, n, dim=axis))(v_cache[:, :, Sk - W:, :], rep, axis=1)
        scores = torch.einsum("bhqd,bhkd->bhqk", q, k) * (Dh ** -0.5)
        p = _softmax(scores, axis=-1)
        o = torch.einsum("bhqk,bhkd->bhqd", p, v)
        return (lambda x, *d: x.permute(*d))(o, 0, 2, 1, 3).reshape(B, Sq, Hq * Dh) @ wo

    def _B_ref(x, w1, b1, w2, b2):
        y = _layernorm(x, axis=-1)
        h = _gelu(torch.einsum("bsd,df->bsf", y, w1) + b1)
        return torch.einsum("bsf,fd->bsd", h, w2) + b2

    x = A_x
    x = x + _A_ref(_layernorm(x, axis=-1), A_k_cache, A_v_cache, A_wq, A_wo)
    x = x + _B_ref(_layernorm(x, axis=-1), B_w1, B_b1, B_w2, B_b2)
    x = x + _A_ref(_layernorm(x, axis=-1), A_k_cache, A_v_cache, A_wq, A_wo)
    x = x + _B_ref(_layernorm(x, axis=-1), B_w1, B_b1, B_w2, B_b2)
    return x
```

```cpp
#include <hip/hip_runtime.h>
#include <hip/hip_bf16.h>
#include <stdint.h>
#include <type_traits>
#include <utility>

typedef unsigned u32;
typedef unsigned long long u64;
typedef unsigned short u16;
typedef u32 u32x4 __attribute__((ext_vector_type(4)));
typedef u32 u32x2 __attribute__((ext_vector_type(2)));
typedef u64 u64x4 __attribute__((ext_vector_type(4)));
typedef u32 u32x8 __attribute__((ext_vector_type(8)));
typedef float f32x4 __attribute__((ext_vector_type(4)));
typedef float f32x2 __attribute__((ext_vector_type(2)));
typedef short s16x4 __attribute__((ext_vector_type(4)));
typedef __bf16 bf16x2 __attribute__((ext_vector_type(2)));
#define GAS __attribute__((address_space(1)))
#define LAS __attribute__((address_space(3)))
#define DI __device__ __forceinline__

namespace {

constexpr int DM = 640, FF = 1728;
constexpr u32 SENT = 0xFFFFFFFFu;
constexpr u32 SPIN_CAP = 1u << 18;
constexpr float LN_EPS = 1e-5f;
constexpr float SC_L2E = 0.08838834764831845f * 1.4426950408889634f;
constexpr u32 ONES = 0x3F803F80u;

constexpr int WS_EPOCH = 0, WS_FLAGS = 256;
constexpr int WS_S = 16384;
constexpr int LG_ONE = 0;
constexpr int LG_XM = 16384;
constexpr int LG_HINT = LG_XM + 10240;
constexpr int LG_SP = 32768;
constexpr int LG_O = 49152;
constexpr int LG_XA = LG_O + 131072;
constexpr int LG_H = LG_XA + 40960;
constexpr int LG_Q1 = 278528;
constexpr int LG_END = LG_Q1 + 32 * 8192;
static_assert(LG_H + 55296 <= LG_Q1 && LG_Q1 % 8192 == 0 && LG_END == 528 * 1024, "hand-off region");
constexpr int WS_Q0 = WS_S + 2 * LG_END;
constexpr int WS_BYTES = WS_Q0 + 32 * 9 * 1536;
DI int lg(int o) { return WS_S + o + (o & ~8191); }
constexpr int WS_PROBE = 1900544;
static_assert(WS_BYTES <= WS_PROBE && WS_PROBE % 16384 == 0, "probe");
constexpr int OOB = 1 << 30;

constexpr int NVL = 38;
constexpr int L_SCR = 4 * NVL * 1024;
constexpr int L_P = L_SCR, L_MRG = L_SCR + 4096, L_ML = L_SCR + 4064, L_ST = L_SCR + 6144, L_QCS = L_SCR + 6656, L_CS = L_SCR + 7168;
constexpr int LDS_BYTES = L_SCR + 8192;
constexpr int L_DUMP = L_SCR + 7936;
static_assert(LDS_BYTES <= 163840, "maps");

enum { R_WO = 0, R_W2 = 1, R_WQ = 2, R_W1 = 3, R_NONE = 4 };

struct Args {
  const char* wp[4];
  char* ws; const char* kc; const char* vc; const u16* x; const u16* b1; const u16* b2; u16* out;
};

DI float bflo(u32 v) { return __builtin_bit_cast(float, v << 16); }
DI float bfhi(u32 v) { return __builtin_bit_cast(float, v & 0xffff0000u); }
DI u32 pack_bf16(float lo, float hi) { f32x2 v = {lo, hi}; return __builtin_bit_cast(u32, __builtin_convertvector(v, bf16x2)); }
DI u32 nosent(u32 v) { return v < 0xFFFFFFFEu ? v : 0xFFFFFFFEu; }
DI int copy_of(int w, int so, int sd) { return w == 0 ? so + (sd ^ 8192) : w == 1 ? so + sd : OOB; }
DI int head_of(int s) { return ((s & 7) << 2) | (s >> 3); }
DI float dot2(u32 a, u32 b, float c) { return __builtin_amdgcn_fdot2_f32_bf16(__builtin_bit_cast(bf16x2, a), __builtin_bit_cast(bf16x2, b), c, false); }
template <int CTRL> DI u32 dppu(u32 x) { return (u32)__builtin_amdgcn_mov_dpp((int)x, CTRL, 0xf, 0xf, true); }
#define WRED(op) "s_nop 2\n\t" op " %0, %0, %0 quad_perm:[1,0,3,2] row_mask:0xf bank_mask:0xf\n\ts_nop 1\n\t" op " %0, %0, %0 quad_perm:[2,3,0,1] row_mask:0xf bank_mask:0xf\n\t" \
  "s_nop 1\n\t" op " %0, %0, %0 row_ror:4 row_mask:0xf bank_mask:0xf\n\ts_nop 1\n\t" op " %0, %0, %0 row_ror:8 row_mask:0xf bank_mask:0xf\n\t" \
  "s_nop 1\n\t" op " %0, %0, %0 row_bcast:15 row_mask:0xa bank_mask:0xf\n\ts_nop 1\n\t" op " %0, %0, %0 row_bcast:31 row_mask:0xc bank_mask:0xf\n\ts_nop 0"
DI float rl63(float x) { return __builtin_bit_cast(float, __builtin_amdgcn_readlane(__builtin_bit_cast(int, x), 63)); }
DI float wave_max(float x) { asm(WRED("v_max_f32_dpp") : "+v"(x)); return rl63(x); }
DI float wave_sum(float x) { asm(WRED("v_add_f32_dpp") : "+v"(x)); return rl63(x); }
DI int lane_id() { int l; asm volatile("v_mbcnt_lo_u32_b32 %0, -1, 0\n\tv_mbcnt_hi_u32_b32 %0, -1, %0" : "=v"(l)); return l; }
#define PIN(x) asm volatile("" : "+v"(x))
#define SPIN(x) asm volatile("" : "+s"(x))

#define BAR() asm volatile("s_waitcnt lgkmcnt(0)\n\ts_barrier" ::: "memory")
#define VMWAIT(n) asm volatile("s_waitcnt vmcnt(%0)" ::"i"(n) : "memory")
#define MF "v_mfma_f32_16x16x32_bf16 "
#define MFMA_AV(z, d, a, b) do { if (z) asm volatile(MF "%0, %1, %2, 0" : "=&v"(d) : "a"(a), "v"(b)); else asm volatile(MF "%0, %1, %2, %0" : "+v"(d) : "a"(a), "v"(b)); } while (0)
#define MFMA_VV(z, d, a, b) do { if (z) asm volatile(MF "%0, %1, %2, 0" : "=&v"(d) : "v"(a), "v"(b)); else asm volatile(MF "%0, %1, %2, %0" : "+v"(d) : "v"(a), "v"(b)); } while (0)
#define MFMA_VN(z, d, a, b) do { if (z) asm volatile("s_nop 1\n\t" MF "%0, %1, %2, 0" : "=&v"(d) : "v"(a), "v"(b)); else asm volatile("s_nop 1\n\t" MF "%0, %1, %2, %0" : "+v"(d) : "v"(a), "v"(b)); } while (0)
#define LD_A(d, voff, sb, imm) asm volatile("global_load_dwordx4 %0, %1, %2 offset:%3 nt" : "=a"(d) : "v"(voff), "s"(sb), "i"(imm) : "memory")
#define LD_B(d, voff, rs, so, pol) asm volatile("buffer_load_dwordx4 %0, %1, %2, %3 offen" pol : "=v"(d) : "v"(voff), "s"(rs), "s"(so) : "memory")


template <int C0 = 0, int C1 = 4> DI void vtrans(u32x4 (&r)[8]) {
#pragma unroll
  for (int c = C0; c < C1; ++c)
#pragma unroll
    for (int a = 0; a <= c; ++a) {
      if (a == c) {
        u32 x0 = r[2 * a][a], x1 = r[2 * a + 1][a], t;
        asm("v_perm_b32 %2, %1, %0, %3\n\tv_perm_b32 %1, %1, %0, %4\n\tv_mov_b32 %0, %2" : "+v"(x0), "+v"(x1), "=&v"(t) : "s"(0x05040100u), "s"(0x07060302u));
        r[2 * a][a] = x0; r[2 * a + 1][a] = x1;
      } else {
        u32 x0 = r[2 * a][c], x1 = r[2 * a + 1][c], y0 = r[2 * c][a], y1 = r[2 * c + 1][a], t0, t1;
        asm("v_perm_b32 %4, %1, %0, %6\n\tv_perm_b32 %5, %1, %0, %7\n\tv_perm_b32 %0, %3, %2, %6\n\tv_perm_b32 %1, %3, %2, %7\n\t"
            "v_mov_b32 %2, %4\n\tv_mov_b32 %3, %5"
            : "+v"(x0), "+v"(x1), "+v"(y0), "+v"(y1), "=&v"(t0), "=&v"(t1) : "s"(0x05040100u), "s"(0x07060302u));
        r[2 * a][c] = x0; r[2 * a + 1][c] = x1; r[2 * c][a] = y0; r[2 * c + 1][a] = y1;
      }
    }
}

template <int N> DI void g_issue(__amdgpu_buffer_rsrc_t rs, int voff, const int (&so)[N], u32x4 (&v)[8]) {
  asm volatile(".if %c[n] > 0\n\tbuffer_load_dwordx4 v[224:227], %[vo], %[rs], %[s0] offen sc1\n\t.endif\n\t" ".if %c[n] > 1\n\tbuffer_load_dwordx4 v[228:231], %[vo], %[rs], %[s1] offen sc1\n\t.endif\n\t" ".if %c[n] > 2\n\tbuffer_load_dwordx4 v[232:235], %[vo], %[rs], %[s2] offen sc1\n\t.endif\n\t" ".if %c[n] > 3\n\tbuffer_load_dwordx4 v[236:239], %[vo], %[rs], %[s3] offen sc1\n\t.endif\n\t" ".if %c[n] > 4\n\tbuffer_load_dwordx4 v[240:243], %[vo], %[rs], %[s4] offen sc1\n\t.endif\n\t" ".if %c[n] > 5\n\tbuffer_load_dwordx4 v[244:247], %[vo], %[rs], %[s5] offen sc1\n\t.endif\n\t" ".if %c[n] > 6\n\tbuffer_load_dwordx4 v[248:251], %[vo], %[rs], %[s6] offen sc1\n\t.endif\n\t" ".if %c[n] > 7\n\tbuffer_load_dwordx4 v[252:255], %[vo], %[rs], %[s7] offen sc1\n\t.endif\n\t"
               : "=&{v[224:227]}"(v[0]), "=&{v[228:231]}"(v[1]), "=&{v[232:235]}"(v[2]), "=&{v[236:239]}"(v[3]), "=&{v[240:243]}"(v[4]), "=&{v[244:247]}"(v[5]), "=&{v[248:251]}"(v[6]), "=&{v[252:255]}"(v[7])
               : [vo] "v"(voff), [rs] "s"(rs), [s0] "s"(so[0 < N ? 0 : 0]), [s1] "s"(so[1 < N ? 1 : 0]), [s2] "s"(so[2 < N ? 2 : 0]), [s3] "s"(so[3 < N ? 3 : 0]), [s4] "s"(so[4 < N ? 4 : 0]), [s5] "s"(so[5 < N ? 5 : 0]), [s6] "s"(so[6 < N ? 6 : 0]), [s7] "s"(so[7 < N ? 7 : 0]), [n] "i"(N) : "memory");
}
template <int I, int N> DI void g_wait(__amdgpu_buffer_rsrc_t rs, int voff, const int (&so)[N], u32x4 (&v)[8]) {
  u32 t, cnt;
  asm volatile("s_waitcnt vmcnt(%c[n]-1-%c[i])\n\t" "v_max_u32 %[t], v[224+4*%c[i]], v[225+4*%c[i]]\n\tv_max3_u32 %[t], %[t], v[226+4*%c[i]], v[227+4*%c[i]]\n\tv_cmp_eq_u32 vcc, -1, %[t]\n\ts_cbranch_vccz .Lgd%=\n\t"
               "s_mov_b32 %[c], %[cap]\n.Lgl%=:\n\t"
               ".if %c[i] <= 0 && %c[n] > 0\n\tbuffer_load_dwordx4 v[224:227], %[vo], %[rs], %[s0] offen sc1\n\t.endif\n\t" ".if %c[i] <= 1 && %c[n] > 1\n\tbuffer_load_dwordx4 v[228:231], %[vo], %[rs], %[s1] offen sc1\n\t.endif\n\t" ".if %c[i] <= 2 && %c[n] > 2\n\tbuffer_load_dwordx4 v[232:235], %[vo], %[rs], %[s2] offen sc1\n\t.endif\n\t" ".if %c[i] <= 3 && %c[n] > 3\n\tbuffer_load_dwordx4 v[236:239], %[vo], %[rs], %[s3] offen sc1\n\t.endif\n\t" ".if %c[i] <= 4 && %c[n] > 4\n\tbuffer_load_dwordx4 v[240:243], %[vo], %[rs], %[s4] offen sc1\n\t.endif\n\t" ".if %c[i] <= 5 && %c[n] > 5\n\tbuffer_load_dwordx4 v[244:247], %[vo], %[rs], %[s5] offen sc1\n\t.endif\n\t" ".if %c[i] <= 6 && %c[n] > 6\n\tbuffer_load_dwordx4 v[248:251], %[vo], %[rs], %[s6] offen sc1\n\t.endif\n\t" ".if %c[i] <= 7 && %c[n] > 7\n\tbuffer_load_dwordx4 v[252:255], %[vo], %[rs], %[s7] offen sc1\n\t.endif\n\t"
               "s_waitcnt vmcnt(%c[n]-1-%c[i])\n\t" "v_max_u32 %[t], v[224+4*%c[i]], v[225+4*%c[i]]\n\tv_max3_u32 %[t], %[t], v[226+4*%c[i]], v[227+4*%c[i]]\n\tv_cmp_eq_u32 vcc, -1, %[t]\n\ts_cbranch_vccz .Lgd%=\n\t"
               "s_sub_u32 %[c], %[c], 1\n\ts_cbranch_scc0 .Lgl%=\n.Lgd%=:"
               : "+{v[224:227]}"(v[0]), "+{v[228:231]}"(v[1]), "+{v[232:235]}"(v[2]), "+{v[236:239]}"(v[3]), "+{v[240:243]}"(v[4]), "+{v[244:247]}"(v[5]), "+{v[248:251]}"(v[6]), "+{v[252:255]}"(v[7]), [t] "=&v"(t), [c] "=&s"(cnt)
               : [vo] "v"(voff), [rs] "s"(rs), [s0] "s"(so[0 < N ? 0 : 0]), [s1] "s"(so[1 < N ? 1 : 0]), [s2] "s"(so[2 < N ? 2 : 0]), [s3] "s"(so[3 < N ? 3 : 0]), [s4] "s"(so[4 < N ? 4 : 0]), [s5] "s"(so[5 < N ? 5 : 0]), [s6] "s"(so[6 < N ? 6 : 0]), [s7] "s"(so[7 < N ? 7 : 0]), [n] "i"(N), [i] "i"(I), [cap] "i"(SPIN_CAP)
               : "memory", "vcc", "scc");
}
template <int I, int N, class F> DI void gather_from(__amdgpu_buffer_rsrc_t rs, int voff, const int (&so)[N], u32x4 (&v)[8], F&& use) {
  if constexpr (I < N) {
    g_wait<I, N>(rs, voff, so, v);
    use(std::integral_constant<int, I>{}, v[I]);
    gather_from<I + 1, N>(rs, voff, so, v, use);
  }
}
template <int N, class F> DI void gather_use(__amdgpu_buffer_rsrc_t rs, int voff, const int (&so)[N], F&& use) {
  u32x4 v[8];
  g_issue<N>(rs, voff, so, v);
  gather_from<0, N>(rs, voff, so, v, use);
}
DI void gather_q(__amdgpu_buffer_rsrc_t rs, int voff, int voff2, int s0, int s2, f32x2& a, f32x2& st) {
  u32 t, cnt;
  f32x2 b, c;
  asm volatile("s_mov_b32 %[c], %[cap]\n.Lgl%=:\n\t"
               "buffer_load_dwordx2 v[248:249], %[v2], %[rs], %[s2] offen sc1\n\tbuffer_load_dwordx2 v[250:251], %[vo], %[rs], %[s0] offen sc1\n\t"
               "buffer_load_dwordx2 v[252:253], %[vo], %[rs], %[s0] offen offset:512 sc1\n\tbuffer_load_dwordx2 v[254:255], %[vo], %[rs], %[s0] offen offset:1024 sc1\n\t"
               "s_waitcnt vmcnt(2)\n\tv_max_u32 %[t], v248, v249\n\tv_max3_u32 %[t], %[t], v250, v251\n\t"
               "v_add_f32_dpp v248, v248, v248 quad_perm:[1,0,3,2] row_mask:0xf bank_mask:0xf\n\tv_add_f32_dpp v249, v249, v249 quad_perm:[1,0,3,2] row_mask:0xf bank_mask:0xf\n\t"
               "s_waitcnt vmcnt(1)\n\tv_max3_u32 %[t], %[t], v252, v253\n\tv_pk_add_f32 v[250:251], v[250:251], v[252:253]\n\t"
               "v_add_f32_dpp v248, v248, v248 quad_perm:[2,3,0,1] row_mask:0xf bank_mask:0xf\n\tv_add_f32_dpp v249, v249, v249 quad_perm:[2,3,0,1] row_mask:0xf bank_mask:0xf\n\t"
               "s_waitcnt vmcnt(0)\n\tv_max3_u32 %[t], %[t], v254, v255\n\tv_cmp_eq_u32 vcc, -1, %[t]\n\tv_pk_add_f32 v[250:251], v[250:251], v[254:255]\n\t"
               "s_cbranch_vccz .Lgd%=\n\ts_sub_u32 %[c], %[c], 1\n\ts_cbranch_scc0 .Lgl%=\n.Lgd%=:"
               : "=&{v[250:251]}"(a), "=&{v[248:249]}"(st), "=&{v[252:253]}"(b), "=&{v[254:255]}"(c), [t] "=&v"(t), [c] "=&s"(cnt)
               : [vo] "v"(voff), [v2] "v"(voff2), [rs] "s"(rs), [s0] "s"(s0), [s2] "s"(s2), [cap] "i"(SPIN_CAP) : "memory", "vcc", "scc");
}
#define GSLEEP 3
template <int SL = GSLEEP> DI u32 gather1(__amdgpu_buffer_rsrc_t rs, int voff, int sbase) {
  u32 v, cnt;
  asm volatile("s_mov_b32 %[c], %[cap]\n.Lgl%=:\n\t"
               "buffer_load_dword %[v], %[vo], %[rs], %[s0] offen sc1\n\ts_waitcnt vmcnt(0)\n\tv_cmp_eq_u32 vcc, -1, %[v]\n\t"
               "s_cbranch_vccz .Lgd%=\n\ts_sleep %c[sl]\n\ts_sub_u32 %[c], %[c], 1\n\ts_cbranch_scc0 .Lgl%=\n.Lgd%=:"
               : [v] "=&v"(v), [c] "=&s"(cnt) : [vo] "v"(voff), [rs] "s"(rs), [s0] "s"(sbase), [cap] "i"(SPIN_CAP), [sl] "i"(SL) : "memory", "vcc", "scc");
  return v;
}
template <int NAPN, int SL> DI void hint_wait(__amdgpu_buffer_rsrc_t rs, int sbase) {
  u32 v, cnt;
  asm volatile("buffer_load_dword %[v], off, %[rs], %[s0] sc1\n\ts_mov_b32 %[c], %[cap]\n\ts_waitcnt vmcnt(0)\n\tv_cmp_eq_u32 vcc, -1, %[v]\n\ts_cbranch_vccz .Lgd%=\n"
               ".Lgl%=:\n\tbuffer_load_dword %[v], off, %[rs], %[s0] sc1\n\ts_waitcnt vmcnt(0)\n\tv_cmp_eq_u32 vcc, -1, %[v]\n\t"
               "s_cbranch_vccz .Lge%=\n\t.if %c[sl]\n\ts_sleep %c[sl]\n\t.endif\n\ts_sub_u32 %[c], %[c], 1\n\ts_cbranch_scc0 .Lgl%=\n.Lge%=:\n\t.if %c[nap]\n\ts_sleep %c[nap]\n\t.endif\n.Lgd%=:"
               : [v] "=&v"(v), [c] "=&s"(cnt) : [rs] "s"(rs), [s0] "s"(sbase), [cap] "i"(SPIN_CAP), [nap] "i"(NAPN), [sl] "i"(SL) : "memory", "vcc", "scc");
}
DI u32x2 gather_pair(__amdgpu_buffer_rsrc_t rs, int voff, int sbase) {
  u32 a, b, t, cnt;
  asm volatile("s_mov_b32 %[c], %[cap]\n.Lgl%=:\n\t"
               "buffer_load_dword %[a], %[vo], %[rs], %[s0] offen sc1 ; slowpath\n\tbuffer_load_dword %[b], %[vo], %[rs], %[s0] offen offset:128 sc1 ; slowpath\n\t"
               "s_waitcnt vmcnt(0) ; slowpath\n\tv_max_u32 %[t], %[a], %[b]\n\tv_cmp_eq_u32 vcc, -1, %[t]\n\t"
               "s_cbranch_vccz .Lgd%=\n\ts_sleep 3\n\ts_sub_u32 %[c], %[c], 1\n\ts_cbranch_scc0 .Lgl%=\n.Lgd%=:"
               : [a] "=&v"(a), [b] "=&v"(b), [t] "=&v"(t), [c] "=&s"(cnt) : [vo] "v"(voff), [rs] "s"(rs), [s0] "s"(sbase), [cap] "i"(SPIN_CAP) : "memory", "vcc", "scc");
  return u32x2{a, b};
}
#define D_H 6
#define D_R 12
#define D_X 10
#define HINT_PREP(stage, cond) int hso = (cond) && cg < 8 ? lg(LG_HINT) + ((stage) * 8 + cg) * 128 : OOB, hvo = lane < 2 ? lane * 8192 : OOB; SPIN(hso); PIN(hvo)
#define HINT_PUT() __builtin_amdgcn_raw_buffer_store_b32((u32)hvo, rs, hvo, hso, 16)
#define SPIN_ALL(a, n_) _Pragma("unroll") for (int i_ = 0; i_ < (n_); ++i_) SPIN(a[i_])
#define HINT_GET(prev, stage, nap) do { if (w == 0) { if ((prev) >= 0) hint_wait<0, GSLEEP>(rs, lg(LG_HINT) + sd + ((prev) * 8 + (blk & 7)) * 128); \
    hint_wait<nap, 0>(rs, lg(LG_HINT) + sd + ((stage) * 8 + (blk & 7)) * 128); } asm volatile("s_barrier" ::: "memory"); } while (0)

template <int K> DI void qadd(float& t, float d, float c) {
  if constexpr (K == 0) asm volatile("v_add_f32_dpp %0, %1, %2 quad_perm:[0,0,0,0] row_mask:0xf bank_mask:0xf" : "=v"(t) : "v"(d), "v"(c));
  if constexpr (K == 1) asm volatile("v_add_f32_dpp %0, %1, %2 quad_perm:[1,1,1,1] row_mask:0xf bank_mask:0xf" : "=v"(t) : "v"(d), "v"(c));
  if constexpr (K == 2) asm volatile("v_add_f32_dpp %0, %1, %2 quad_perm:[2,2,2,2] row_mask:0xf bank_mask:0xf" : "=v"(t) : "v"(d), "v"(c));
  if constexpr (K == 3) asm volatile("v_add_f32_dpp %0, %1, %2 quad_perm:[3,3,3,3] row_mask:0xf bank_mask:0xf" : "=v"(t) : "v"(d), "v"(c));
}
template <int E, int STEP> DI void qk_extract(const f32x4& d, float& t, float (&s)[4]) {
  constexpr int r = E & 3;
  if constexpr (STEP == 0) qadd<(r + 1) & 3>(t, d[(r + 1) & 3], d[r]);
  if constexpr (STEP == 1) qadd<(r + 2) & 3>(t, d[(r + 2) & 3], t);
  if constexpr (STEP == 2) { if constexpr (r == 0) qadd<3>(s[E >> 2], d[3], t); else qadd<(r + 3) & 3>(t, d[(r + 3) & 3], t); }
  if constexpr (STEP == 3 && r != 0) asm volatile("v_cndmask_b32 %0, %0, %1, %2" : "+v"(s[E >> 2]) : "v"(t), "s"(0x1111111111111111ull << r));
}
template <int G, class F> DI void qk_group(const u32x4 (&kf)[64], u32 q, u32x4 (&qb)[4], f32x4 (&d)[3], float& t, float (&s)[4], F&& hook) {
  if constexpr (G < 16) {
    constexpr int S = G >> 2, r = G & 3;
    auto step = [&](auto J) {
      constexpr int j = J.value;
      if constexpr (G == 0) {
        u32 a0 = qb[j][0], a1 = qb[j][1], a2 = qb[j][2], a3 = qb[j][3];
        if constexpr (j == 0) asm volatile("s_nop 1\n\tv_mov_b32_dpp %0, %4 quad_perm:[0,1,2,3] row_mask:0xf bank_mask:0x1\n\tv_mov_b32_dpp %1, %4 row_ror:12 row_mask:0xf bank_mask:0x1\n\tv_mov_b32_dpp %2, %4 row_ror:8 row_mask:0xf bank_mask:0x1\n\tv_mov_b32_dpp %3, %4 row_ror:4 row_mask:0xf bank_mask:0x1\n\ts_nop 1" : "+v"(a0), "+v"(a1), "+v"(a2), "+v"(a3) : "v"(q));
        if constexpr (j == 1) asm volatile("v_mov_b32_dpp %0, %4 row_ror:4 row_mask:0xf bank_mask:0x2\n\tv_mov_b32_dpp %1, %4 quad_perm:[0,1,2,3] row_mask:0xf bank_mask:0x2\n\tv_mov_b32_dpp %2, %4 row_ror:12 row_mask:0xf bank_mask:0x2\n\tv_mov_b32_dpp %3, %4 row_ror:8 row_mask:0xf bank_mask:0x2\n\ts_nop 1" : "+v"(a0), "+v"(a1), "+v"(a2), "+v"(a3) : "v"(q));
        if constexpr (j == 2) asm volatile("v_mov_b32_dpp %0, %4 row_ror:8 row_mask:0xf bank_mask:0x4\n\tv_mov_b32_dpp %1, %4 row_ror:4 row_mask:0xf bank_mask:0x4\n\tv_mov_b32_dpp %2, %4 quad_perm:[0,1,2,3] row_mask:0xf bank_mask:0x4\n\tv_mov_b32_dpp %3, %4 row_ror:12 row_mask:0xf bank_mask:0x4\n\ts_nop 1" : "+v"(a0), "+v"(a1), "+v"(a2), "+v"(a3) : "v"(q));
        if constexpr (j == 3) asm volatile("v_mov_b32_dpp %0, %4 row_ror:12 row_mask:0xf bank_mask:0x8\n\tv_mov_b32_dpp %1, %4 row_ror:8 row_mask:0xf bank_mask:0x8\n\tv_mov_b32_dpp %2, %4 row_ror:4 row_mask:0xf bank_mask:0x8\n\tv_mov_b32_dpp %3, %4 quad_perm:[0,1,2,3] row_mask:0xf bank_mask:0x8\n\ts_nop 1" : "+v"(a0), "+v"(a1), "+v"(a2), "+v"(a3) : "v"(q));
        qb[j] = u32x4{a0, a1, a2, a3};
      }
      MFMA_AV(j == 0, d[G % 3], kf[16 * j + 4 * r + S], qb[j]);
      if constexpr (G == 1) asm volatile("s_nop 0");
      if constexpr (G >= 2) { qk_extract<G - 2, j>(d[(G - 2) % 3], t, s); if constexpr (((G - 2) & 3) == 0 && j == 3) asm volatile("s_nop 0"); }
    };
    step(std::integral_constant<int, 0>{}); step(std::integral_constant<int, 1>{}); step(std::integral_constant<int, 2>{}); step(std::integral_constant<int, 3>{});
    hook(std::integral_constant<int, G>{});
    qk_group<G + 1>(kf, q, qb, d, t, s, hook);
  } else {
    qk_extract<14, 0>(d[14 % 3], t, s); qk_extract<14, 1>(d[14 % 3], t, s); qk_extract<14, 2>(d[14 % 3], t, s); qk_extract<14, 3>(d[14 % 3], t, s);
    asm volatile("s_nop 2");
    qk_extract<15, 0>(d[15 % 3], t, s); qk_extract<15, 1>(d[15 % 3], t, s); qk_extract<15, 2>(d[15 % 3], t, s); qk_extract<15, 3>(d[15 % 3], t, s);
  }
}
template <class F> DI void qk(const u32x4 (&kf)[64], u32 q, u32x4 (&qb)[4], float (&s)[4], F&& hook) {
  f32x4 d[3];
  float t;
  qk_group<0>(kf, q, qb, d, t, s, hook);
}

template <int P> DI u32x4 pbcast(u32 p01, u32 p23) {
  return u32x4{dppu<0x150 + 2 * P>(p01), dppu<0x150 + 2 * P>(p23), dppu<0x150 + 2 * P + 1>(p01), dppu<0x150 + 2 * P + 1>(p23)};
}

struct Soft { u32 p01, p23; float m; };
DI Soft softmax(const float (&s)[4], float c) {
  Soft r;
  float mx;
  asm("v_max3_f32 %0, %1, %2, %3\n\tv_max_f32 %0, %0, %4" : "=&v"(mx) : "v"(s[0]), "v"(s[1]), "v"(s[2]), "v"(s[3]));
  r.m = wave_max(mx) * c;
  const float p0 = __builtin_amdgcn_exp2f(fmaf(s[0], c, -r.m)), p1 = __builtin_amdgcn_exp2f(fmaf(s[1], c, -r.m));
  const float p2 = __builtin_amdgcn_exp2f(fmaf(s[2], c, -r.m)), p3 = __builtin_amdgcn_exp2f(fmaf(s[3], c, -r.m));
  r.p01 = pack_bf16(p0, p1); r.p23 = pack_bf16(p2, p3);
  return r;
}
DI void ml_write(LAS char* L, int w, const Soft& sm) {
  const float l = wave_sum(dot2(sm.p01, ONES, dot2(sm.p23, ONES, 0.f)));
  *(LAS f32x2*)(L + L_ML + w * 8) = f32x2{sm.m, l};
}
DI float ml_factor(LAS const char* L, const Soft& sm) {
  const f32x4 a = *(LAS const f32x4*)(L + L_ML), b = *(LAS const f32x4*)(L + L_ML + 16);
  const float M = fmaxf(fmaxf(a[0], a[2]), fmaxf(b[0], b[2]));
  const float den = __builtin_amdgcn_exp2f(a[0] - M) * a[1] + __builtin_amdgcn_exp2f(a[2] - M) * a[3] + __builtin_amdgcn_exp2f(b[0] - M) * b[1] + __builtin_amdgcn_exp2f(b[2] - M) * b[3];
  return __builtin_amdgcn_exp2f(sm.m - M) * __builtin_amdgcn_rcpf(den);
}
DI void sum_publish(LAS char* L, __amdgpu_buffer_rsrc_t rs, int wr, int rd, f32x2 e, int sdst) {
  *(LAS f32x2*)(L + wr) = e;
  BAR();
  f32x2 pv[4];
#pragma unroll
  for (int i = 0; i < 4; ++i) pv[i] = *(LAS const f32x2*)(L + rd + i * 512);
  const f32x2 r = (pv[0] + pv[1]) + (pv[2] + pv[3]);
  __builtin_amdgcn_raw_buffer_store_b32(nosent(pack_bf16(r[0], r[1])), rs, rd >> 1, sdst, 16);
}
template <class F, int... I> DI void for_seq(F&& f, std::integer_sequence<int, I...>) { (f(std::integral_constant<int, I>{}), ...); }
DI void merge_publish(LAS char* L, __amdgpu_buffer_rsrc_t rs, int wr, int rd, const f32x4 (&o)[8], float f, int sdst) {
  float e0, e1;
#define RM(d, k, m) "v_mul_f32_dpp " d ", " k ", %10 quad_perm:[0,1,2,3] row_mask:" m " bank_mask:0xf\n\t"
  asm volatile("s_nop 0\n\t" RM("%0", "%2", "0x1") RM("%1", "%3", "0x1") RM("%0", "%4", "0x2") RM("%1", "%5", "0x2") RM("%0", "%6", "0x4") RM("%1", "%7", "0x4") RM("%0", "%8", "0x8") RM("%1", "%9", "0x8")
               : "=&v"(e0), "=&v"(e1) : "v"(o[0][0]), "v"(o[1][0]), "v"(o[2][0]), "v"(o[3][0]), "v"(o[4][0]), "v"(o[5][0]), "v"(o[6][0]), "v"(o[7][0]), "v"(f),
                 "v"(o[0]), "v"(o[1]), "v"(o[2]), "v"(o[3]), "v"(o[4]), "v"(o[5]), "v"(o[6]), "v"(o[7]));
  sum_publish(L, rs, wr, rd, f32x2{e0, e1}, sdst);
}

DI f32x2 row_stats(const u32x4& a, const u32x4& b) {
  float s1 = 0.f, s2 = 0.f;
#pragma unroll
  for (int i = 0; i < 4; ++i) { s1 = dot2(a[i], ONES, s1); s2 = dot2(a[i], a[i], s2); s1 = dot2(b[i], ONES, s1); s2 = dot2(b[i], b[i], s2); }
  const float mu = wave_sum(s1) * (1.0f / DM);
  const float var = fmaxf(wave_sum(s2) * (1.0f / DM) - mu * mu, 0.f);
  return f32x2{mu, __builtin_amdgcn_rsqf(var + LN_EPS)};
}
template <bool CS> DI void wq_publish(__amdgpu_buffer_rsrc_t rs, f32x4 (&acc)[2], int vo, int sdst) {
  if (CS) asm volatile("s_nop 7" : "+v"(acc[0]), "+v"(acc[1])); else asm volatile("" : "+v"(acc[0]), "+v"(acc[1]));
#pragma unroll
  for (int i = 0; i < 4; ++i) {
    auto x = __builtin_amdgcn_permlane16_swap(__float_as_uint(acc[0][i]), __float_as_uint(acc[1][i]), false, false);
    auto y = __builtin_amdgcn_permlane32_swap(x[0], x[1], false, false);
    if (CS) __builtin_amdgcn_raw_buffer_store_b32(y[0], rs, vo, sdst + i * 1536, 16); else __builtin_amdgcn_raw_buffer_store_b32(nosent(y[0]), rs, vo, sdst + i * 1536, 16);
    if (CS && i == 0) __builtin_amdgcn_raw_buffer_store_b32(nosent(y[1]), rs, vo < 6144 ? vo : OOB, sdst + 8 * 1536, 16);
  }
}
template <int PART> DI void quad_reduce(f32x4 (&c)[4]) {
  if constexpr (PART == 1)
    asm volatile("v_permlane32_swap_b32 v0, v8\n\tv_permlane32_swap_b32 v4, v12\n\t" "v_permlane32_swap_b32 v1, v9\n\tv_permlane32_swap_b32 v5, v13\n\t" "v_permlane32_swap_b32 v2, v10\n\tv_permlane32_swap_b32 v6, v14\n\t" "v_permlane32_swap_b32 v3, v11\n\tv_permlane32_swap_b32 v7, v15\n\t"
                 "v_add_f32_dpp v0, v8, v0 quad_perm:[2,3,0,1] row_mask:0xf bank_mask:0xf\n\t" "v_add_f32_dpp v4, v12, v4 quad_perm:[2,3,0,1] row_mask:0xf bank_mask:0xf\n\t" "v_add_f32_dpp v1, v9, v1 quad_perm:[2,3,0,1] row_mask:0xf bank_mask:0xf\n\t" "v_add_f32_dpp v5, v13, v5 quad_perm:[2,3,0,1] row_mask:0xf bank_mask:0xf\n\t"
                 : "+{v[0:3]}"(c[0]), "+{v[4:7]}"(c[1]), "+{v[8:11]}"(c[2]), "+{v[12:15]}"(c[3]));
  if constexpr (PART == 2)
    asm volatile("v_add_f32_dpp v2, v10, v2 quad_perm:[2,3,0,1] row_mask:0xf bank_mask:0xf\n\t" "v_add_f32_dpp v6, v14, v6 quad_perm:[2,3,0,1] row_mask:0xf bank_mask:0xf\n\t" "v_add_f32_dpp v3, v11, v3 quad_perm:[2,3,0,1] row_mask:0xf bank_mask:0xf\n\t" "v_add_f32_dpp v7, v15, v7 quad_perm:[2,3,0,1] row_mask:0xf bank_mask:0xf\n\t"
                 "v_permlane16_swap_b32 v0, v4\n\t" "v_permlane16_swap_b32 v1, v5\n\t" "v_permlane16_swap_b32 v2, v6\n\t" "v_permlane16_swap_b32 v3, v7\n\t"
                 "v_add_f32_dpp v0, v4, v0 quad_perm:[1,0,3,2] row_mask:0xf bank_mask:0xf\n\t" "v_add_f32_dpp v1, v5, v1 quad_perm:[1,0,3,2] row_mask:0xf bank_mask:0xf\n\t" "v_add_f32_dpp v2, v6, v2 quad_perm:[1,0,3,2] row_mask:0xf bank_mask:0xf\n\t" "v_add_f32_dpp v3, v7, v3 quad_perm:[1,0,3,2] row_mask:0xf bank_mask:0xf\n\t"
                 : "+{v[0:3]}"(c[0]), "+{v[4:7]}"(c[1]), "+{v[8:11]}"(c[2]), "+{v[12:15]}"(c[3]));
  if constexpr (PART == 3)
    asm volatile("v_permlane32_swap_b32 v16, v24\n\tv_permlane32_swap_b32 v20, v28\n\t" "v_permlane32_swap_b32 v17, v25\n\tv_permlane32_swap_b32 v21, v29\n\t" "v_permlane32_swap_b32 v18, v26\n\tv_permlane32_swap_b32 v22, v30\n\t" "v_permlane32_swap_b32 v19, v27\n\tv_permlane32_swap_b32 v23, v31\n\t"
                 "v_add_f32_dpp v16, v24, v16 quad_perm:[2,3,0,1] row_mask:0xf bank_mask:0xf\n\t" "v_add_f32_dpp v20, v28, v20 quad_perm:[2,3,0,1] row_mask:0xf bank_mask:0xf\n\t" "v_add_f32_dpp v17, v25, v17 quad_perm:[2,3,0,1] row_mask:0xf bank_mask:0xf\n\t" "v_add_f32_dpp v21, v29, v21 quad_perm:[2,3,0,1] row_mask:0xf bank_mask:0xf\n\t"
                 "v_add_f32_dpp v18, v26, v18 quad_perm:[2,3,0,1] row_mask:0xf bank_mask:0xf\n\t" "v_add_f32_dpp v22, v30, v22 quad_perm:[2,3,0,1] row_mask:0xf bank_mask:0xf\n\t" "v_add_f32_dpp v19, v27, v19 quad_perm:[2,3,0,1] row_mask:0xf bank_mask:0xf\n\t" "v_add_f32_dpp v23, v31, v23 quad_perm:[2,3,0,1] row_mask:0xf bank_mask:0xf\n\t"
                 "v_permlane16_swap_b32 v16, v20\n\t" "v_permlane16_swap_b32 v17, v21\n\t" "v_permlane16_swap_b32 v18, v22\n\t" "v_permlane16_swap_b32 v19, v23\n\t"
                 "v_add_f32_dpp v16, v20, v16 quad_perm:[1,0,3,2] row_mask:0xf bank_mask:0xf\n\t" "v_add_f32_dpp v17, v21, v17 quad_perm:[1,0,3,2] row_mask:0xf bank_mask:0xf\n\t" "v_add_f32_dpp v18, v22, v18 quad_perm:[1,0,3,2] row_mask:0xf bank_mask:0xf\n\t" "v_add_f32_dpp v19, v23, v19 quad_perm:[1,0,3,2] row_mask:0xf bank_mask:0xf\n\t"
                 : "+{v[16:19]}"(c[0]), "+{v[20:23]}"(c[1]), "+{v[24:27]}"(c[2]), "+{v[28:31]}"(c[3]));
}
DI u32x4 tr_frag(LAS char* s0, LAS char* s1) {
  const u32x2 a0 = __builtin_bit_cast(u32x2, __builtin_amdgcn_ds_read_tr16_b64_v4i16((LAS s16x4*)s0));
  const u32x2 a1 = __builtin_bit_cast(u32x2, __builtin_amdgcn_ds_read_tr16_b64_v4i16((LAS s16x4*)s1));
  return u32x4{a0.x, a0.y, a1.x, a1.y};
}
DI void load_if_zero(u32x4& d, int c, int voff, __amdgpu_buffer_rsrc_t rs, int so) {
  asm volatile("s_cmp_lg_u32 %1, 0\n\ts_cbranch_scc1 .Lws%=\n\tbuffer_load_dwordx4 %0, %2, %3, %4 offen ; optload\n.Lws%=:"
               : "+v"(d) : "s"(c), "v"(voff), "s"(rs), "s"(so) : "memory", "scc");
}
constexpr bool v_in_lds(int P, int t) { return (P & 3) < 2 || ((P & 3) == 2 && t % 3 == 1); }
constexpr int v_slot(int P, int t) { return ((P & 3) < 2 ? (P & 3) * 8 + t : 16 + t / 3) * 2 + (P >> 2); }
constexpr int v_reg(int P, int t) { return (P & 3) == 3 ? t : 8 + t - (t + 1) / 3; }
constexpr int pv_p(int f) { return f < 16 ? (f & 1) * 3 : 1 + (f & 1); }
constexpr int pv_t(int f) { return ((f >> 1) + (f & 1) * 4) & 7; }
constexpr int pv_k(int f) { int k = 0; for (int i = 0; i < f; ++i) k += v_in_lds(pv_p(i), pv_t(i)); return k; }
constexpr int pv_slot(int k) { for (int i = 0; i < 32; ++i) if (v_in_lds(pv_p(i), pv_t(i)) && pv_k(i) == k) return v_slot(pv_p(i), pv_t(i)); return 0; }
DI u32x8 cat8(u32x4 a, u32x4 b) { return __builtin_shufflevector(a, b, 0, 1, 2, 3, 4, 5, 6, 7); }
DI u32x8 lds_pair(LAS const char* p) { return cat8(*(LAS const u32x4*)p, *(LAS const u32x4*)(p + 1024)); }
template <int K> DI u32 shl(u32 x) { if constexpr (K == 0) return x; else return dppu<0x100 + K>(x); }
template <int P> DI u32x4 sp_a(u32 lo, u32 hi) { return u32x4{shl<2 * P>(lo), shl<2 * P + 1>(lo), shl<2 * P>(hi), shl<2 * P + 1>(hi)}; }
#define SMFMA(d, a, b, ix) asm volatile("v_smfmac_f32_16x16x64_bf16 %0, %1, %2, %3" : "+v"(d) : "v"(a), "v"(b), "v"(ix))
#define SOFT_T0 500
#define SOFT_DT 638
template <int K> DI void soft(u32 t0) {
  u64 t;
  asm volatile("s_memrealtime %0\n\ts_waitcnt lgkmcnt(0)" : "=s"(t) :: "memory");
  if ((u32)t - t0 < (SOFT_T0 + K * SOFT_DT) / 10) asm volatile("s_waitcnt vmcnt(2) ; slowpath" ::: "memory");
}
template <int BUF, int K> DI void ld_v4(u32x4 (&r)[8], u32 voff, const char* p0, const char* p1, u32 t0) {
  if constexpr (BUF == 0) {
    soft<K + 0>(t0);
    asm volatile("global_load_dwordx4 %0, %1, %2 offset:0 nt" : "={v[120:123]}"(r[0]) : "v"(voff), "s"(p0) : "memory");
    asm volatile("global_load_dwordx4 %0, %1, %2 offset:1024 nt" : "={v[224:227]}"(r[1]) : "v"(voff), "s"(p0) : "memory");
    asm volatile("global_load_dwordx4 %0, %1, %2 offset:2048 nt" : "={v[128:131]}"(r[2]) : "v"(voff), "s"(p0) : "memory");
    asm volatile("global_load_dwordx4 %0, %1, %2 offset:3072 nt" : "={v[136:139]}"(r[3]) : "v"(voff), "s"(p0) : "memory");
    soft<K + 1>(t0);
    asm volatile("global_load_dwordx4 %0, %1, %2 offset:0 nt" : "={v[228:231]}"(r[4]) : "v"(voff), "s"(p1) : "memory");
    asm volatile("global_load_dwordx4 %0, %1, %2 offset:1024 nt" : "={v[144:147]}"(r[5]) : "v"(voff), "s"(p1) : "memory");
    asm volatile("global_load_dwordx4 %0, %1, %2 offset:2048 nt" : "={v[152:155]}"(r[6]) : "v"(voff), "s"(p1) : "memory");
    asm volatile("global_load_dwordx4 %0, %1, %2 offset:3072 nt" : "={v[232:235]}"(r[7]) : "v"(voff), "s"(p1) : "memory");
  }
  if constexpr (BUF == 1) {
    soft<K + 0>(t0);
    asm volatile("global_load_dwordx4 %0, %1, %2 offset:0 nt" : "={v[124:127]}"(r[0]) : "v"(voff), "s"(p0) : "memory");
    asm volatile("global_load_dwordx4 %0, %1, %2 offset:1024 nt" : "={v[236:239]}"(r[1]) : "v"(voff), "s"(p0) : "memory");
    asm volatile("global_load_dwordx4 %0, %1, %2 offset:2048 nt" : "={v[132:135]}"(r[2]) : "v"(voff), "s"(p0) : "memory");
    asm volatile("global_load_dwordx4 %0, %1, %2 offset:3072 nt" : "={v[140:143]}"(r[3]) : "v"(voff), "s"(p0) : "memory");
    soft<K + 1>(t0);
    asm volatile("global_load_dwordx4 %0, %1, %2 offset:0 nt" : "={v[240:243]}"(r[4]) : "v"(voff), "s"(p1) : "memory");
    asm volatile("global_load_dwordx4 %0, %1, %2 offset:1024 nt" : "={v[148:151]}"(r[5]) : "v"(voff), "s"(p1) : "memory");
    asm volatile("global_load_dwordx4 %0, %1, %2 offset:2048 nt" : "={v[156:159]}"(r[6]) : "v"(voff), "s"(p1) : "memory");
    asm volatile("global_load_dwordx4 %0, %1, %2 offset:3072 nt" : "={v[244:247]}"(r[7]) : "v"(voff), "s"(p1) : "memory");
  }
  if constexpr (BUF == 2) {
    soft<K + 0>(t0);
    asm volatile("global_load_dwordx4 %0, %1, %2 offset:0 nt" : "={v[56:59]}"(r[0]) : "v"(voff), "s"(p0) : "memory");
    asm volatile("global_load_dwordx4 %0, %1, %2 offset:1024 nt" : "={v[64:67]}"(r[1]) : "v"(voff), "s"(p0) : "memory");
    asm volatile("global_load_dwordx4 %0, %1, %2 offset:2048 nt" : "={v[72:75]}"(r[2]) : "v"(voff), "s"(p0) : "memory");
    asm volatile("global_load_dwordx4 %0, %1, %2 offset:3072 nt" : "={v[80:83]}"(r[3]) : "v"(voff), "s"(p0) : "memory");
    soft<K + 1>(t0);
    asm volatile("global_load_dwordx4 %0, %1, %2 offset:0 nt" : "={v[88:91]}"(r[4]) : "v"(voff), "s"(p1) : "memory");
    asm volatile("global_load_dwordx4 %0, %1, %2 offset:1024 nt" : "={v[96:99]}"(r[5]) : "v"(voff), "s"(p1) : "memory");
    asm volatile("global_load_dwordx4 %0, %1, %2 offset:2048 nt" : "={v[104:107]}"(r[6]) : "v"(voff), "s"(p1) : "memory");
    asm volatile("global_load_dwordx4 %0, %1, %2 offset:3072 nt" : "={v[112:115]}"(r[7]) : "v"(voff), "s"(p1) : "memory");
  }
  if constexpr (BUF == 3) {
    soft<K + 0>(t0);
    asm volatile("global_load_dwordx4 %0, %1, %2 offset:0 nt" : "={v[60:63]}"(r[0]) : "v"(voff), "s"(p0) : "memory");
    asm volatile("global_load_dwordx4 %0, %1, %2 offset:1024 nt" : "={v[68:71]}"(r[1]) : "v"(voff), "s"(p0) : "memory");
    asm volatile("global_load_dwordx4 %0, %1, %2 offset:2048 nt" : "={v[76:79]}"(r[2]) : "v"(voff), "s"(p0) : "memory");
    asm volatile("global_load_dwordx4 %0, %1, %2 offset:3072 nt" : "={v[84:87]}"(r[3]) : "v"(voff), "s"(p0) : "memory");
    soft<K + 1>(t0);
    asm volatile("global_load_dwordx4 %0, %1, %2 offset:0 nt" : "={v[92:95]}"(r[4]) : "v"(voff), "s"(p1) : "memory");
    asm volatile("global_load_dwordx4 %0, %1, %2 offset:1024 nt" : "={v[100:103]}"(r[5]) : "v"(voff), "s"(p1) : "memory");
    asm volatile("global_load_dwordx4 %0, %1, %2 offset:2048 nt" : "={v[108:111]}"(r[6]) : "v"(voff), "s"(p1) : "memory");
    asm volatile("global_load_dwordx4 %0, %1, %2 offset:3072 nt" : "={v[116:119]}"(r[7]) : "v"(voff), "s"(p1) : "memory");
  }
}
template <int BUF, int N> DI void tie_v(u32x4 (&r)[8]) {
  if constexpr (BUF == 0) asm volatile(".if %c8 < 64\n\ts_waitcnt vmcnt(%c8)\n\t.else\n\ts_nop 1\n\t.endif" : "+{v[120:123]}"(r[0]), "+{v[224:227]}"(r[1]), "+{v[128:131]}"(r[2]), "+{v[136:139]}"(r[3]), "+{v[228:231]}"(r[4]), "+{v[144:147]}"(r[5]), "+{v[152:155]}"(r[6]), "+{v[232:235]}"(r[7]) : "i"(N) : "memory");
  if constexpr (BUF == 1) asm volatile(".if %c8 < 64\n\ts_waitcnt vmcnt(%c8)\n\t.else\n\ts_nop 1\n\t.endif" : "+{v[124:127]}"(r[0]), "+{v[236:239]}"(r[1]), "+{v[132:135]}"(r[2]), "+{v[140:143]}"(r[3]), "+{v[240:243]}"(r[4]), "+{v[148:151]}"(r[5]), "+{v[156:159]}"(r[6]), "+{v[244:247]}"(r[7]) : "i"(N) : "memory");
  if constexpr (BUF == 2) asm volatile(".if %c8 < 64\n\ts_waitcnt vmcnt(%c8)\n\t.else\n\ts_nop 1\n\t.endif" : "+{v[56:59]}"(r[0]), "+{v[64:67]}"(r[1]), "+{v[72:75]}"(r[2]), "+{v[80:83]}"(r[3]), "+{v[88:91]}"(r[4]), "+{v[96:99]}"(r[5]), "+{v[104:107]}"(r[6]), "+{v[112:115]}"(r[7]) : "i"(N) : "memory");
  if constexpr (BUF == 3) asm volatile(".if %c8 < 64\n\ts_waitcnt vmcnt(%c8)\n\t.else\n\ts_nop 1\n\t.endif" : "+{v[60:63]}"(r[0]), "+{v[68:71]}"(r[1]), "+{v[76:79]}"(r[2]), "+{v[84:87]}"(r[3]), "+{v[92:95]}"(r[4]), "+{v[100:103]}"(r[5]), "+{v[108:111]}"(r[6]), "+{v[116:119]}"(r[7]) : "i"(N) : "memory");
}
DI void pin_vp(u32x8 (&v)[13]) {
  asm volatile("" : "+{v[56:63]}"(v[0]), "+{v[64:71]}"(v[1]), "+{v[72:79]}"(v[2]), "+{v[80:87]}"(v[3]), "+{v[88:95]}"(v[4]), "+{v[96:103]}"(v[5]), "+{v[104:111]}"(v[6]), "+{v[112:119]}"(v[7]), "+{v[120:127]}"(v[8]), "+{v[128:135]}"(v[9]), "+{v[136:143]}"(v[10]), "+{v[144:151]}"(v[11]), "+{v[152:159]}"(v[12]));
}
DI void pin_wf(u32x4 (&v)[16]) {
  asm volatile("" : "+{v[160:163]}"(v[0]), "+{v[164:167]}"(v[1]), "+{v[168:171]}"(v[2]), "+{v[172:175]}"(v[3]), "+{v[176:179]}"(v[4]), "+{v[180:183]}"(v[5]), "+{v[184:187]}"(v[6]), "+{v[188:191]}"(v[7]), "+{v[192:195]}"(v[8]), "+{v[196:199]}"(v[9]), "+{v[200:203]}"(v[10]), "+{v[204:207]}"(v[11]), "+{v[208:211]}"(v[12]), "+{v[212:215]}"(v[13]), "+{v[216:219]}"(v[14]), "+{v[220:223]}"(v[15]));
}
constexpr int v_buf(int P) { return (P & 1) * 2 + (P >> 2); }
constexpr int v_pos(int P) { return (P & 3) * 2 + (P >> 2); }
constexpr int v_blk(int i) { return (i >> 1) + (i & 1) * 4; }
template <int T> DI void zero_o(f32x4& o) {
  if constexpr (T == 0) asm volatile("v_mov_b32 v224, 0" : "={v[224:227]}"(o));
  if constexpr (T == 1) asm volatile("v_mov_b32 v228, 0" : "={v[228:231]}"(o));
  if constexpr (T == 2) asm volatile("v_mov_b32 v232, 0" : "={v[232:235]}"(o));
  if constexpr (T == 3) asm volatile("v_mov_b32 v236, 0" : "={v[236:239]}"(o));
  if constexpr (T == 4) asm volatile("v_mov_b32 v240, 0" : "={v[240:243]}"(o));
  if constexpr (T == 5) asm volatile("v_mov_b32 v244, 0" : "={v[244:247]}"(o));
  if constexpr (T == 6) asm volatile("v_mov_b32 v248, 0" : "={v[248:251]}"(o));
  if constexpr (T == 7) asm volatile("v_mov_b32 v252, 0" : "={v[252:255]}"(o));
}
constexpr float GELU_K = -2.0f * 1.4426950408889634f * 0.7978845608028654f;
DI float gelu_tanh(float x, float k) {
  const float u2 = x * fmaf(x * x, GELU_K * 0.044715f, k);
  return x * __builtin_amdgcn_rcpf(1.0f + __builtin_amdgcn_exp2f(u2));
}

__global__ void __launch_bounds__(256, 1) fused_kernel(Args a) {
  LAS char* L = (LAS char*)(u32)0;
  const int tid = threadIdx.x, lane = tid & 63, w = __builtin_amdgcn_readfirstlane(tid >> 6), blk = blockIdx.x;
  const int g = lane >> 4, n = lane & 15;
#define XMAP 0x24736150u
  const int jj = blk >> 3, mem = jj & 3;
  const int xcl = (XMAP >> (4 * (blk & 7))) & 7;
  const int grp = xcl * 4 + ((jj >> 2) & 3), hi = grp >= 10 ? 1 : 0;
  const int rl = xcl >= 5 ? R_WQ : jj < 16 ? R_WO : hi ? R_W2 : R_W1;
  u64 wbase, wob;
  u64x4 ka;
  asm volatile("s_load_dwordx2 %0, %3, %4\n\ts_load_dwordx8 %1, %3, 0x20\n\ts_load_dwordx2 %2, %3, 0x0"
               : "=&s"(wbase), "=&s"(ka), "=&s"(wob) : "s"(__builtin_amdgcn_kernarg_segment_ptr()), "s"(rl * 8));
  const bool idle = rl == R_W1 && grp == 9;
  const int role = idle ? R_NONE : rl;
  const int part = hi;
  const int q3 = xcl - 5, qh = jj;
  const int cg = rl == R_WQ ? jj : (grp - 10 * hi) * 4 + mem;
  u32 lin = lane * 16, qperm = (n >> 2) * 256 + g * 64 + (n & 3) * 16;
  u64 kvoff = (u64)((blk & ~31) + head_of(blk & 31)) * 1048576 + 786432 + (u64)(w * 4096);
  asm volatile("" : "+v"(lin), "+v"(qperm));
  asm volatile("s_waitcnt lgkmcnt(0)" : "+s"(wbase), "+s"(ka), "+s"(wob), "+s"(kvoff));
  char* const ws = (char*)ka[0];
  const u16* const xin = (const u16*)ka[3];
  const int bb = blk >> 5, hh = blk & 31;
  const char* kb = (const char*)ka[1] + kvoff;
  const char* vb = (const char*)ka[2] + kvoff;
  const __amdgpu_buffer_rsrc_t rs = __builtin_amdgcn_make_buffer_rsrc(ws, 0, WS_BYTES, 0x00020000);

  u32x4 kf[64], vraw[8][8], wf[16], x0[7], xr[2], fl[2];
  f32x2 qr[6];
  u32x8 vp[13];
  u32x2 epv;
  u32 pf[3];
#define KS1 1
#define KS2 3
#define TILE(t) ((t) * 16384)
#define ISSUE_V(P) do { ld_v4<v_buf(P), 16 + 2 * v_pos(P)>(vraw[P], lin, vb + TILE(2 * v_pos(P)), vb + TILE(2 * v_pos(P) + 1), tsoft); } while (0)
#define ISSUE_K1(t_) do { const char* p_ = kb + TILE(t_); if ((t_) >= 4) soft<t_>(tsoft); \
    _Pragma("unroll") for (int s_ = 0; s_ < 4; ++s_) LD_A(kf[8 * v_blk((t_) >> 1) + 4 * ((t_) & 1) + s_], qperm, p_, s_ * 1024); } while (0)
#define IK(t, T0, T1) if ((t) >= (T0) && (t) < (T1)) ISSUE_K1(t);
#define ISSUE_K(T0, T1) do { IK(0,T0,T1) IK(1,T0,T1) IK(2,T0,T1) IK(3,T0,T1) IK(4,T0,T1) IK(5,T0,T1) IK(6,T0,T1) IK(7,T0,T1) IK(8,T0,T1) IK(9,T0,T1) IK(10,T0,T1) IK(11,T0,T1) IK(12,T0,T1) IK(13,T0,T1) IK(14,T0,T1) IK(15,T0,T1) } while (0)
#define AH1 300
#define AH2 180
#define AHO 220
#define AHO2 40
  u32 tsoft = (u32)__builtin_amdgcn_s_memrealtime() - (u32)(rl == R_W1 ? AH1 : rl == R_W2 ? AH2 : rl == R_WO ? AHO : 0);
  ISSUE_K(0, 1);
  u32 lat = 0xFFFF;
  if (w == 0 && rl != R_WQ) {
    u64 t0, t1; u32 d;
    asm volatile("s_load_dword %2, %4, 0\n\ts_waitcnt lgkmcnt(0)\n\t"
                 "s_memtime %0\n\ts_waitcnt lgkmcnt(0)\n\ts_load_dword %2, %3, 0\n\ts_waitcnt lgkmcnt(0)\n\ts_memtime %1\n\ts_waitcnt lgkmcnt(0)"
                 : "=&s"(t0), "=&s"(t1), "=&s"(d) : "s"(ws + WS_PROBE + (blk & 4) * 4096 + (blk & 8) * 1024 + ((blk & 3) * 16 + (blk >> 4)) * 128), "s"(ws + WS_PROBE + 32768));
    lat = (u32)(t1 - t0) < 4095u ? (u32)(t1 - t0) : 4095u;
  }
  const int wby = idle ? 0 : (rl & 1) ? FF * DM * 2 : 4096 * DM * 2;
  const __amdgpu_buffer_rsrc_t rx = __builtin_amdgcn_make_buffer_rsrc((void*)xin, 0, 8 * DM * 2, 0x00020000);
  {
    const __amdgpu_buffer_rsrc_t rq = __builtin_amdgcn_make_buffer_rsrc((void*)wbase, 0, wby, 0x00020000);
    int w0 = (q3 * 224 + w * 56) * 8192 + head_of(qh) * 256;
    const int xv = n < 8 ? n * (2 * DM) + g * 16 + q3 * 448 : OOB;
    asm volatile("s_cmp_lg_u32 %[rl], 2\n\ts_cbranch_scc1 .Lwq%=\n\t"
                 "buffer_load_dwordx4 %0, %[wv], %[rq], %[so] offen nt ; optload\n\ts_add_u32 %[so], %[so], 0x8000\n\t" "buffer_load_dwordx4 %1, %[wv], %[rq], %[so] offen nt ; optload\n\ts_add_u32 %[so], %[so], 0x8000\n\t"
                 "buffer_load_dwordx4 %2, %[wv], %[rq], %[so] offen nt ; optload\n\ts_add_u32 %[so], %[so], 0x8000\n\t" "buffer_load_dwordx4 %3, %[wv], %[rq], %[so] offen nt ; optload\n\ts_add_u32 %[so], %[so], 0x8000\n\t"
                 "buffer_load_dwordx4 %4, %[wv], %[rq], %[so] offen nt ; optload\n\ts_add_u32 %[so], %[so], 0x8000\n\t" "buffer_load_dwordx4 %5, %[wv], %[rq], %[so] offen nt ; optload\n\ts_add_u32 %[so], %[so], 0x8000\n\t"
                 "buffer_load_dwordx4 %6, %[wv], %[rq], %[so] offen nt ; optload\n\ts_add_u32 %[so], %[so], 0x8000\n\t" "buffer_load_dwordx4 %7, %[wv], %[rq], %[so] offen nt ; optload\n\ts_add_u32 %[so], %[so], 0x8000\n\t"
                 "buffer_load_dwordx4 %8, %[wv], %[rq], %[so] offen nt ; optload\n\ts_add_u32 %[so], %[so], 0x8000\n\t" "buffer_load_dwordx4 %9, %[wv], %[rq], %[so] offen nt ; optload\n\ts_add_u32 %[so], %[so], 0x8000\n\t"
                 "buffer_load_dwordx4 %10, %[wv], %[rq], %[so] offen nt ; optload\n\ts_add_u32 %[so], %[so], 0x8000\n\t" "buffer_load_dwordx4 %11, %[wv], %[rq], %[so] offen nt ; optload\n\ts_add_u32 %[so], %[so], 0x8000\n\t"
                 "buffer_load_dwordx4 %12, %[wv], %[rq], %[so] offen nt ; optload\n\ts_add_u32 %[so], %[so], 0x8000\n\t" "buffer_load_dwordx4 %13, %[wv], %[rq], %[so] offen nt ; optload\n\ts_add_u32 %[so], %[so], 0x8000\n\t"
                 "buffer_load_dwordx4 %14, %[xv], %[rx], 0 offen offset:0 ; optload\n\t" "buffer_load_dwordx4 %15, %[xv], %[rx], 0 offen offset:64 ; optload\n\t"
                 "buffer_load_dwordx4 %16, %[xv], %[rx], 0 offen offset:128 ; optload\n\t" "buffer_load_dwordx4 %17, %[xv], %[rx], 0 offen offset:192 ; optload\n\t"
                 "buffer_load_dwordx4 %18, %[xv], %[rx], 0 offen offset:256 ; optload\n\t" "buffer_load_dwordx4 %19, %[xv], %[rx], 0 offen offset:320 ; optload\n\t"
                 "buffer_load_dwordx4 %20, %[x6], %[rx], 0 offen offset:384 ; optload\n\t" "\n.Lwq%=:"
                 : "=&v"(wf[0]), "=&v"(wf[1]), "=&v"(wf[2]), "=&v"(wf[3]), "=&v"(wf[4]), "=&v"(wf[5]), "=&v"(wf[6]), "=&v"(wf[7]), "=&v"(wf[8]), "=&v"(wf[9]), "=&v"(wf[10]), "=&v"(wf[11]), "=&v"(wf[12]), "=&v"(wf[13]), "=&v"(x0[0]), "=&v"(x0[1]), "=&v"(x0[2]), "=&v"(x0[3]), "=&v"(x0[4]), "=&v"(x0[5]), "=&v"(x0[6]), [so] "+s"(w0)
                 : [rl] "s"(rl), [wv] "v"(g * 8192 + n * 16), [rq] "s"(rq), [xv] "v"(xv), [x6] "v"(q3 == 2 ? OOB : xv), [rx] "s"(rx) : "memory", "scc");
    wf[14] = wf[15] = u32x4{0u, 0u, 0u, 0u};
    asm volatile("buffer_load_dwordx2 %0, off, %1, 0 sc1" : "=v"(epv) : "s"(rs) : "memory");
    LD_B(xr[0], lin, rx, bb * (2 * DM), "");
    LD_B(xr[1], lane < 16 ? lin : OOB, rx, bb * (2 * DM) + 1024, "");
    u64 pc; u32 len;
    asm volatile("s_getpc_b64 %0\n.Lfk_pc:\n\ts_mov_b32 %1, .Lfk_end-.Lfk_pc" : "=s"(pc), "=s"(len));
    const __amdgpu_buffer_rsrc_t rc = __builtin_amdgcn_make_buffer_rsrc((void*)pc, 0, len, 0x00020000);
    asm volatile("buffer_load_dword %0, %1, %2, %3 offen" : "=v"(pf[0]) : "v"(lane < 16 ? lane * 64 : OOB), "s"(rc), "s"(((jj & 15) * 4 + w) * 1024) : "memory");
    asm volatile("global_load_dword %0, %1, %2 nt" : "=v"(pf[1]) : "v"(0), "s"(vb) : "memory");
    const int pc_ = w * 256 + blk, q6 = ((pc_ - 640) * 171) >> 10;
    const int co = pc_ < 640 ? pc_ * 896 : 2 * LG_Q1 + q6 * 8192 + (pc_ - 640 - 6 * q6) * 1024;
    const u32 cv = pc_ * 896 + lin < 32768 ? ONES : SENT;
    __builtin_amdgcn_raw_buffer_store_b128(u32x4{cv, cv, cv, cv}, rs, pc_ >= 640 || lane < 56 ? lin : OOB, WS_S + co, 16);
  }
  ISSUE_K(1, 4);
  asm volatile("s_waitcnt vmcnt(12)" : "+v"(wf[0]), "+v"(wf[1]), "+v"(wf[2]), "+v"(wf[3]), "+v"(wf[4]), "+v"(wf[5]), "+v"(wf[6]), "+v"(wf[7]),
               "+v"(wf[8]), "+v"(wf[9]), "+v"(wf[10]), "+v"(wf[11]), "+v"(wf[12]), "+v"(wf[13]),
               "+v"(x0[0]), "+v"(x0[1]), "+v"(x0[2]), "+v"(x0[3]), "+v"(x0[4]), "+v"(x0[5]), "+v"(x0[6]), "+v"(xr[0]), "+v"(xr[1]), "+v"(pf[0]), "+v"(pf[1]), "+v"(epv) :: "memory");
  const u64 epoch = (u64)(u32)__builtin_amdgcn_readfirstlane(epv.x) | (u64)(u32)__builtin_amdgcn_readfirstlane(epv.y) << 32;
  const u64 tag64 = (epoch ^ 0x9E3779B97F4A7C15ull) * 0xD6E8FEB86659FD93ull + 0x632BE59BD9B4E019ull;
  const u32 tag = (u32)(tag64 >> 32), tagl = (u32)tag64 >> 16;
  if (role == R_WQ) {
#pragma unroll
    for (int f = 0; f < 14; ++f) *(LAS u32x4*)(L + (w * 14 + f) * 1024 + lin) = wf[f];
  }
  ISSUE_K(4, 4 + KS1);
  if (role == R_WQ) {
    BAR();
    LAS char* src = L + (8 * g + ((lane >> 2) & 3)) * 256 + w * 64 + (lane & 3) * 16;
#pragma unroll
    for (int f = 0; f < 14; ++f) wf[f] = tr_frag(src + (f >> 1) * 8192 + (f & 1) * 8, src + (f >> 1) * 8192 + (f & 1) * 8 + 1024);
    f32x4 acc[2];
#pragma unroll
    for (int s = 0; s < 7; ++s) {
      if (n == 8) x0[s] = u32x4{ONES, ONES, ONES, ONES};
      MFMA_VN(s == 0, acc[0], x0[s], wf[2 * s]); MFMA_VN(s == 0, acc[1], x0[s], wf[2 * s + 1]);
    }
    wq_publish<true>(rs, acc, (lane >> 5) * 6144 + w * 128 + ((lane >> 4) & 1) * 64 + ((lane >> 1) & 1) * 32 + ((lane >> 2) & 3) * 8 + (lane & 1) * 4, WS_Q0 + qh * (9 * 1536) + q3 * 512);
  }
  pin_wf(wf);
  f32x2 mr = row_stats(xr[0], xr[1]);
  ISSUE_K(4 + KS1, 4 + KS1 + KS2);
  VMWAIT(4 * KS2);
  asm volatile("s_barrier" ::: "memory");
  if (tid == 0) __builtin_amdgcn_raw_buffer_store_b64(u32x2{tagl << 16 | lat, tag}, rs, 0, WS_FLAGS + blk * 8, 16);
#define PF_2(rp_, v0_, v1_, so_) do { \
    asm volatile("buffer_load_dword %0, %1, %2, %3 offen" : "=v"(pf[1]) : "v"(v0_), "s"(rp_), "s"(so_) : "memory"); \
    asm volatile("buffer_load_dword %0, %1, %2, %3 offen" : "=v"(pf[2]) : "v"(v1_), "s"(rp_), "s"(so_) : "memory"); } while (0)
#define PFOWN2 80
#define PFOWN 112
  {
    const __amdgpu_buffer_rsrc_t rp = __builtin_amdgcn_make_buffer_rsrc((void*)wob, 0, rl != R_WQ ? 4096 * DM * 2 : 0, 0x00020000);
    const int i0 = ((mem * 4 + w) * 128 + lane) * (2 * DM), i1 = i0 + 64 * 2 * DM;
    const int pfo = hi ? PFOWN2 : grp < 8 ? PFOWN : rl == R_WO || idle ? 64 : 128;
    PF_2(rp, idle ? i1 - 128 : (lane < pfo) == (rl == R_WO) ? i0 : OOB, (lane + 64 < pfo) == (rl == R_WO) ? i1 : OOB, hi * 2048 * 2 * DM + (cg >> 2) * 128);
    const __amdgpu_buffer_rsrc_t rb = __builtin_amdgcn_make_buffer_rsrc((void*)(hi ? a.b2 : a.b1), 0, (rl & 1) && w == 0 && mem == 0 ? (hi ? 2 * DM : 2 * FF) : 0, 0x00020000);
    asm volatile("buffer_load_dword %0, %1, %2, 0 offen" : "=v"(pf[0]) : "v"(lane * 128), "s"(rb) : "memory");
  }
  ISSUE_K(4 + KS1 + KS2, 16);
  LD_B(fl[0], lin, rs, WS_FLAGS, " sc1");
  LD_B(fl[1], lin, rs, WS_FLAGS + 1024, " sc1");
  VMWAIT(40);
  ISSUE_V(0); ISSUE_V(4);
  asm volatile("s_waitcnt vmcnt(16)" : "+v"(fl[0]), "+v"(fl[1]), "+v"(pf[0]), "+v"(pf[1]), "+v"(pf[2]) :: "memory");
  for (u32 spins = 0; spins < SPIN_CAP / 8; ++spins) {
    if (__all(fl[0].x >> 16 == tagl && fl[0].y == tag && fl[0].z >> 16 == tagl && fl[0].w == tag && fl[1].x >> 16 == tagl && fl[1].y == tag && fl[1].z >> 16 == tagl && fl[1].w == tag)) break;
    __builtin_amdgcn_s_sleep(32);
    LD_B(fl[0], lin, rs, WS_FLAGS, " sc1 ; slowpath");
    LD_B(fl[1], lin, rs, WS_FLAGS + 1024, " sc1 ; slowpath");
    asm volatile("s_waitcnt vmcnt(0) ; slowpath" : "+v"(fl[0]), "+v"(fl[1]) :: "memory");
  }
  if (blk == 0 && tid == 0) __builtin_amdgcn_raw_buffer_store_b64(u32x2{(u32)(epoch + 1), (u32)((epoch + 1) >> 32)}, rs, 0, WS_EPOCH, 16);
  const u32 lm = min(min(fl[0].x, fl[0].z), min(fl[1].x, fl[1].z)) & 0xFFFF;
  const float vote = wave_sum(((lane >> 1) ^ (lane >> 2)) & 1 ? (float)lm : -(float)lm);
  const int qs = WS_Q0 + hh * (9 * 1536);
#pragma unroll
  for (int i = 0; i < 6; ++i)
    asm volatile("buffer_load_dwordx2 %0, %1, %2, %3 offen offset:%4 sc1" : "=v"(qr[i]) : "v"(lane * 8), "s"(rs), "s"(qs + (i < 3 ? bb : 8) * 1536), "i"(i % 3 * 512) : "memory");
  ISSUE_V(1);
  asm volatile("s_waitcnt vmcnt(8)" : "+v"(qr[0]), "+v"(qr[1]), "+v"(qr[2]), "+v"(qr[3]), "+v"(qr[4]), "+v"(qr[5]) :: "memory");
  const int pol = vote < 0.f ? 8192 : 0;

  const int sd = ((blk << 11) ^ pol) & 8192;
  const int odst = head_of(hh) * 2048 + bb * 256;
  {
    const f32x2 qcs = qr[3] + qr[4] + qr[5], q2 = qr[0] + qr[1] + qr[2] - mr[0] * qcs;
    if (w == 0) *(LAS f32x2*)(L + L_QCS + lane * 8) = qcs;
    u32x4 qb[4] = {};
    float s[4];
    const __amdgpu_buffer_rsrc_t rwo = __builtin_amdgcn_make_buffer_rsrc((void*)wbase, 0, wby, 0x00020000);
    const int wv = (lane >> 1) * (2 * DM) + (lane & 1) * 16, w0 = (part * 2048 + w * 512) * (2 * DM) + cg * 32;
    qk(kf, pack_bf16(q2[0], q2[1]), qb, s, [&](auto G) { load_if_zero(wf[G.value], role, wv, rwo, w0 + G.value * 32 * (2 * DM)); });
    tsoft += rl == R_WO ? AHO - AHO2 : 0;
    const Soft sm = softmax(s, SC_L2E * mr[1]);
    ml_write(L, w, sm);
    LAS char* vl = L + w * (NVL * 1024) + lane * 16;
    f32x4 o[8];
    float fac;
    u32x4 pa7;
#define PV1(P) do { if ((P) != 7) vtrans(vraw[P]); u32x4 (&t_)[8] = vraw[P]; const u32x4 pa_ = (P) == 7 ? pa7 : pbcast<P>(sm.p01, sm.p23); \
    _Pragma("unroll") for (int i_ = 0; i_ < 8; ++i_) if (v_in_lds(P, i_)) *(LAS u32x4*)(vl + v_slot(P, i_) * 1024) = t_[i_]; \
    asm volatile("" :: "v"(pa_)); tie_v<v_buf(P), 64>(t_); \
    _Pragma("unroll") for (int i_ = 0; i_ < 8; ++i_) MFMA_VV((P) == 0, o[i_], pa_, t_[i_]); } while (0)
    tie_v<v_buf(0), 63>(vraw[0]); PV1(0);
    ISSUE_V(5);
    tie_v<v_buf(4), 63>(vraw[4]); PV1(4); ISSUE_V(2);
    tie_v<v_buf(1), 16>(vraw[1]);
    asm volatile("" : "+v"(wf[0]), "+v"(wf[1]), "+v"(wf[2]), "+v"(wf[3]), "+v"(wf[4]), "+v"(wf[5]), "+v"(wf[6]), "+v"(wf[7]),
                 "+v"(wf[8]), "+v"(wf[9]), "+v"(wf[10]), "+v"(wf[11]), "+v"(wf[12]), "+v"(wf[13]), "+v"(wf[14]), "+v"(wf[15]) :: "memory");
    if (role == R_WO) {
      LAS char* img = L + w * (NVL * 1024) + (NVL - 16) * 1024;
#pragma unroll
      for (int f = 0; f < 16; ++f) *(LAS u32x4*)(img + f * 1024 + lin) = wf[f];
      asm volatile("" ::: "memory");
      LAS char* src = img + (32 * g + 8 * (lane & 3) + ((lane >> 2) & 3)) * 32;
#pragma unroll
      for (int f = 0; f < 16; ++f) wf[f] = tr_frag(src + (f & 3) * 8 + (f >> 2) * 4096, src + (f & 3) * 8 + (f >> 2) * 4096 + 128);
      asm volatile("" ::: "memory");
    }
    PV1(1); ISSUE_V(6);
    tie_v<v_buf(5), 16>(vraw[5]); PV1(5); ISSUE_V(3);
    tie_v<v_buf(2), 16>(vraw[2]); PV1(2); ISSUE_V(7);
    tie_v<v_buf(6), 16>(vraw[6]); PV1(6);
    BAR();
    fac = ml_factor(L, sm);
    int wr = L_MRG + w * 512 + (8 * n + 2 * g) * 4, rd = L_MRG + lane * 8, dst = copy_of(w, lg(LG_O + odst), sd) - L_MRG / 2;
    PIN(fac); PIN(wr); PIN(rd); SPIN(dst);
    tie_v<v_buf(3), 8>(vraw[3]); PV1(3);
    pa7 = pbcast<7>(sm.p01, sm.p23); PIN(pa7);
    tie_v<v_buf(7), 2>(vraw[7]); vtrans<0, 3>(vraw[7]);
    tie_v<v_buf(7), 0>(vraw[7]); vtrans<3, 4>(vraw[7]); PV1(7);
    merge_publish(L, rs, wr, rd, o, fac, dst);
#pragma unroll
    for (int P = 2; P < 4; ++P)
#pragma unroll
      for (int t = 0; t < 8; ++t) if (!v_in_lds(P, t)) vp[v_reg(P, t)] = cat8(vraw[P][t], vraw[P + 4][t]);
    pin_vp(vp);
  }

  if (role == R_W1 || role == R_W2) {
    const int lane = lane_id(), g = lane >> 4, n = lane & 15;
    const __amdgpu_buffer_rsrc_t rw = __builtin_amdgcn_make_buffer_rsrc((void*)wbase, 0, wby, 0x00020000);
    {
      const int i0 = (mem * 4 + w) * 128 + lane, i1 = i0 + 64, r0 = (i0 * 21846) >> 16, r1 = (i1 * 21846) >> 16;
      PF_2(rw, rl == R_W1 ? r0 * (2 * FF) + (i0 - 3 * r0) * 128 : i0 * (2 * DM), rl == R_W1 ? r1 * (2 * FF) + (i1 - 3 * r1) * 128 : i1 * (2 * DM), (cg >> 2) * (rl == R_W1 ? 384 : 128));
    }
    LAS char* img = L + w * (NVL * 1024);
    u32x4 keep[6];
    const int q = (lane >> 2) & 3, p = lane & 3;
    if (role == R_W1) {
      int wv[3];
#pragma unroll
      for (int i = 0; i < 3; ++i) { const int idx = i * 64 + lane, r = (idx * 171) >> 10; wv[i] = r * (2 * FF) + (idx - 6 * r) * 16; }
#pragma unroll
      for (int f = 0; f < 15; ++f) LD_B(wf[f], wv[f % 3], rw, (w * 160 + (f / 3) * 32) * (2 * FF) + cg * 96, "");
#pragma unroll
      for (int i = 0; i < 6; ++i) keep[i] = *(LAS const u32x4*)(img + i * 1024 + lane * 16);
      LAS char* src = img + (8 * g + q) * 96 + 8 * p;
#pragma unroll
      for (int f = 0; f < 15; f += 3) {
        const int o = (f & 1) * 3072;
        asm volatile("s_waitcnt vmcnt(%3)" : "+v"(wf[f]), "+v"(wf[f + 1]), "+v"(wf[f + 2]) : "i"(12 - f) : "memory");
#pragma unroll
        for (int i = 0; i < 3; ++i) *(LAS u32x4*)(img + o + i * 1024 + lane * 16) = wf[f + i];
        asm volatile("" ::: "memory");
#pragma unroll
        for (int i = 0; i < 3; ++i) wf[f + i] = tr_frag(src + o + i * 32, src + o + i * 32 + 384);
        asm volatile("" ::: "memory");
      }
    } else {
      const int wv = (lane >> 1) * (2 * DM) + (lane & 1) * 16;
#pragma unroll
      for (int f = 0; f < 14; ++f) LD_B(wf[f], wv, rw, (w * 448 + f * 32) * (2 * DM) + cg * 32, "");
#pragma unroll
      for (int i = 0; i < 6; ++i) keep[i] = *(LAS const u32x4*)(img + i * 1024 + lane * 16);
      LAS char* src = img + (16 * g + 8 * (p >> 1) + q) * 32 + 8 * (p & 1);
#pragma unroll
      for (int f = 0; f < 14; f += 2) {
        const int o = (f % 6) * 1024;
        asm volatile("s_waitcnt vmcnt(%2)" : "+v"(wf[f]), "+v"(wf[f + 1]) : "i"(12 - f) : "memory");
        *(LAS u32x4*)(img + o + lane * 16) = wf[f];
        *(LAS u32x4*)(img + o + 1024 + lane * 16) = wf[f + 1];
        asm volatile("" ::: "memory");
        wf[f] = tr_frag(src + o, src + o + 128);
        wf[f + 1] = tr_frag(src + o + 16, src + o + 144);
        asm volatile("" ::: "memory");
      }
    }
#pragma unroll
    for (int i = 0; i < 6; ++i) *(LAS u32x4*)(img + i * 1024 + lane * 16) = keep[i];
    if (role == R_W1) {
    f32x4 cs[3];
    u32x4 ones = {ONES, ONES, ONES, ONES};
#pragma unroll
    for (int s = 0; s < 5; ++s)
#pragma unroll
      for (int t = 0; t < 3; ++t) MFMA_VN(s == 0, cs[t], wf[3 * s + t], ones);
    asm volatile("s_nop 7" : "+v"(cs[0]), "+v"(cs[1]), "+v"(cs[2]));
    if (n == 0) {
#pragma unroll
      for (int t = 0; t < 3; ++t) *(LAS f32x4*)(L + L_CS + (w * 48 + t * 16 + 4 * g) * 4) = cs[t];
    }
      BAR();
    }
    asm volatile("" : "+v"(pf[1]), "+v"(pf[2]));
  }
  pin_wf(wf);
  const int w_o = w, cg_o = cg, q3_o = q3;
#pragma unroll 1
  for (int layer = 0;; ++layer) {
    int w = w_o, cg = cg_o, q3 = q3_o, blk = blockIdx.x;
    asm volatile("" : "+s"(w), "+s"(cg), "+s"(q3), "+s"(blk));
    const int qh = cg, bb = blk >> 5, hh = blk & 31, odst = head_of(hh) * 2048 + bb * 256;
    const int lane = lane_id(), tid = w * 64 + lane, g = lane >> 4, n = lane & 15;
    if (role == R_WO) {
      u32 bia = 0;
      if (w < 2 && part == 0) {
        if (layer == 0) bia = *(const GAS u32*)(xin + ((lane & 31) >> 2) * DM + cg * 16 + (lane >> 5) * 8 + (lane & 3) * 2);
        else bia = __builtin_amdgcn_raw_buffer_load_b32(rs, lane * 4, lg(LG_XM + cg * 256) + sd, 16);
      }
      int so[8];
#pragma unroll
      for (int i = 0; i < 8; ++i) so[i] = lg(LG_O + layer * 65536 + (part * 16 + w * 4) * 2048) + sd + (i & 3) * 2048 + (i >> 2) * 1024;
      int wr = (n & 3) == 0 ? L_P + w * 512 + (g >> 1) * 256 + (n >> 2) * 32 + (g & 1) * 16 : L_DUMP, rd = L_P + lane * 8, sv = (lane >> 5) * 256 + (lane & 31) * 4;
      int dst = copy_of(w, lg(LG_XA + layer * 20480 + cg * 512 + part * 128), sd);
      PIN(wr); PIN(rd); PIN(sv); SPIN(dst);
      HINT_PREP(layer, w == 2 && part == 0);
      f32x4 acc[2][4];
      f32x2 res;
      gather_use<8>(rs, (n >> 2) * 256 + g * 64 + (n & 3) * 16, so, [&](auto I, const u32x4& v) {
        constexpr int i = I.value;
        if constexpr (i == 7) HINT_PUT();
#pragma unroll
        for (int c = 0; c < 4; ++c) MFMA_VV((i & 3) == 0, acc[i >> 2][c], wf[4 * (i & 3) + c], v);
        if constexpr (i == 5) quad_reduce<1>(acc[0]);
        if constexpr (i == 6) { quad_reduce<2>(acc[0]); *(LAS f32x4*)(L + wr) = acc[0][0]; }
      });
      asm volatile("v_lshlrev_b32 %0, 16, %6\n\tv_and_b32 %1, 0xffff0000, %6\n\ts_nop 5"
                   : "=&v"(res[0]), "=&v"(res[1]), "+v"(acc[1][0]), "+v"(acc[1][1]), "+v"(acc[1][2]), "+v"(acc[1][3]) : "v"(bia));
      quad_reduce<3>(acc[1]);
      *(LAS f32x4*)(L + wr + 128) = acc[1][0];
      if (layer == 1 && __any(bia == SENT)) { bia = gather1(rs, lane * 4, lg(LG_XM + cg * 256) + sd); res = f32x2{bflo(bia), bfhi(bia)}; }
      BAR();
      f32x2 p[4];
#pragma unroll
      for (int i = 0; i < 4; ++i) p[i] = *(LAS const f32x2*)(L + rd + i * 512);
      const f32x2 r = ((p[0] + res) + p[1]) + (p[2] + p[3]);
      __builtin_amdgcn_raw_buffer_store_b32(nosent(pack_bf16(r[0], r[1])), rs, sv, dst, 16);
    } else if (role == R_W1) {
      int so[5];
#pragma unroll
      for (int i = 0; i < 5; ++i) so[i] = lg(LG_XA + layer * 20480 + w * 5120 + i * 1024) + sd;
      const int b = (tid & 31) >> 2;
      u32 bia = 0;
      f32x2 cs = {0.f, 0.f};
      if (w < 3) {
        bia = *(const GAS u32*)(a.b1 + cg * 48 + (tid >> 5) * 8 + (tid & 3) * 2);
#pragma unroll
        for (int p = 0; p < 4; ++p) cs += *(LAS const f32x2*)(L + L_CS + (p * 48 + (tid >> 5) * 8 + (tid & 3) * 2) * 4);
      }
      f32x2 bi2 = {bflo(bia), bfhi(bia)};
      float gk = GELU_K;
      PIN(cs); PIN(bi2); PIN(gk);
      const int wb = L_P + ((w * 8 + (n & 7)) * 48 + 4 * g) * 4;
      int wr = wb + (n >> 3) * 64, wr2 = n < 8 ? wb + 128 : g == ((n >> 2) & 1) ? L_ST + (w * 8 + (n & 7)) * 16 : L_DUMP;
      int rd = L_P + (b * 48 + (tid >> 5) * 8 + (tid & 3) * 2) * 4, rt = L_ST + b * 16, sv = lane * 4, gv = lane * 16;
      int dst = w < 3 ? lg(LG_H + layer * 27648 + cg * 768 + w * 256) + sd : OOB, dst2 = dst ^ 8192;
      u32x4 ones = {ONES, ONES, ONES, ONES};
      PIN(wr); PIN(wr2); PIN(rd); PIN(rt); PIN(sv); PIN(gv); PIN(ones); SPIN(dst); SPIN(dst2); SPIN_ALL(so, 5);
      HINT_PREP(2 + layer, w == 3);
      HINT_GET(-1, layer, D_X);
      f32x4 acc[3], sx, sq;
      gather_use<5>(rs, gv, so, [&](auto I, const u32x4& v) {
        constexpr int s = I.value;
        if constexpr (s == 4) HINT_PUT();
        MFMA_VV(s == 0, sq, v, v); MFMA_VV(s == 0, sx, ones, v);
#pragma unroll
        for (int t = 0; t < 3; ++t) MFMA_VV(s == 0, acc[t], wf[3 * s + t], v);
      });
      float m[10];
      asm volatile("s_nop 3\n\tv_cndmask_b32 %8, %22, %23, %27\n\tv_cndmask_b32 %9, %24, %25, %27\n\t" "v_add_f32_dpp %4, %26, %26 row_ror:8 row_mask:0xf bank_mask:0xc\n\t" "v_cndmask_b32 %8, %8, %9, %28\n\t"
                   "v_add_f32_dpp %0, %10, %10 row_ror:8 row_mask:0xf bank_mask:0x3\n\t" "v_add_f32_dpp %1, %11, %11 row_ror:8 row_mask:0xf bank_mask:0x3\n\t" "v_add_f32_dpp %8, %8, %8 row_ror:8 row_mask:0xf bank_mask:0xf\n\t" "v_add_f32_dpp %2, %12, %12 row_ror:8 row_mask:0xf bank_mask:0x3\n\t" "v_mov_b32 %9, %8\n\t" "v_add_f32_dpp %3, %13, %13 row_ror:8 row_mask:0xf bank_mask:0x3\n\t" "v_add_f32_dpp %0, %14, %14 row_ror:8 row_mask:0xf bank_mask:0xc\n\t"
                   "v_permlane32_swap_b32 %8, %9\n\t" "v_add_f32_dpp %1, %15, %15 row_ror:8 row_mask:0xf bank_mask:0xc\n\t" "v_add_f32_dpp %2, %16, %16 row_ror:8 row_mask:0xf bank_mask:0xc\n\t" "v_add_f32_dpp %3, %17, %17 row_ror:8 row_mask:0xf bank_mask:0xc\n\t" "v_add_f32 %5, %8, %9\n\t"
                   "v_add_f32_dpp %4, %18, %18 row_ror:8 row_mask:0xf bank_mask:0x3\n\t" "v_add_f32_dpp %5, %19, %19 row_ror:8 row_mask:0xf bank_mask:0x3\n\t" "v_add_f32_dpp %6, %20, %20 row_ror:8 row_mask:0xf bank_mask:0x3\n\t" "v_add_f32_dpp %7, %21, %21 row_ror:8 row_mask:0xf bank_mask:0x3\n\t"
                   : "=&v"(m[0]), "=&v"(m[1]), "=&v"(m[2]), "=&v"(m[3]), "=&v"(m[4]), "=&v"(m[5]), "=&v"(m[6]), "=&v"(m[7]), "=&v"(m[8]), "=&v"(m[9])
                   : "v"(acc[0][0]), "v"(acc[0][1]), "v"(acc[0][2]), "v"(acc[0][3]), "v"(acc[1][0]), "v"(acc[1][1]), "v"(acc[1][2]), "v"(acc[1][3]),
                     "v"(acc[2][0]), "v"(acc[2][1]), "v"(acc[2][2]), "v"(acc[2][3]), "v"(sq[0]), "v"(sq[1]), "v"(sq[2]), "v"(sq[3]), "v"(sx[0]),
                     "s"(0xAAAAAAAAAAAAAAAAull), "s"(0xCCCCCCCCCCCCCCCCull), "v"(sx));
      *(LAS f32x4*)(L + wr) = f32x4{m[0], m[1], m[2], m[3]};
      *(LAS f32x4*)(L + wr2) = f32x4{m[4], m[5], m[6], m[7]};
      BAR();
      f32x2 p[4], t[4];
#pragma unroll
      for (int i = 0; i < 4; ++i) { t[i] = *(LAS const f32x2*)(L + rt + i * 128); p[i] = *(LAS const f32x2*)(L + rd + i * 1536); }
      const f32x2 sv2 = ((t[0] + t[1]) + (t[2] + t[3])) * (1.0f / DM), av = (p[0] + p[1]) + (p[2] + p[3]);
      const float mu = sv2[0], var = fmaxf(sv2[1] - mu * mu, 0.f);
      const float r = __builtin_amdgcn_rsqf(fmaf(var, 1.0f + LN_EPS, LN_EPS * LN_EPS));
      const float y0 = fmaf(r, fmaf(-mu, cs[0], av[0]), bi2[0]), y1 = fmaf(r, fmaf(-mu, cs[1], av[1]), bi2[1]);
      const u32 y = nosent(pack_bf16(gelu_tanh(y0, gk), gelu_tanh(y1, gk)));
      __builtin_amdgcn_raw_buffer_store_b32(y, rs, sv, dst2, 16);
      __builtin_amdgcn_raw_buffer_store_b32(y, rs, sv, dst, 16);
    } else if (role == R_W2) {
      const u32 bia = *(const GAS u32*)(a.b2 + cg * 16 + (lane >> 5) * 8 + (lane & 3) * 2);
      int ro = (lane >> 5) * 256 + (lane & 31) * 4, rb = lg(LG_XA + layer * 20480 + cg * 512) + sd;
      int so[7];
#pragma unroll
      for (int i = 0; i < 7; ++i) so[i] = w == 3 && i == 6 ? OOB : lg(LG_H + layer * 27648 + w * 7168 + i * 1024) + sd;
      int wr = (g >> 1) == (n >> 3) ? L_P + ((w * 2 + (n >> 3)) * 128 + (n & 7) * 8 + 4 * (g & 1)) * 4 : L_DUMP - 256, rd = L_P + lane * 8, gv = lane * 16;
      int wr0 = (g >> 1) == (n >> 3) ? wr : L_DUMP;
      int sv = layer == 0 ? lane * 4 : (((lane & 31) >> 2) * DM + cg * 16 + (lane >> 5) * 8 + (lane & 3) * 2) * 2;
      int dst = layer == 0 ? copy_of(w - 1, lg(LG_XM + cg * 256), sd) : w == 1 ? 0 : OOB;
      const __amdgpu_buffer_rsrc_t rdst = layer == 0 ? rs : __builtin_amdgcn_make_buffer_rsrc((void*)a.out, 0, 8 * DM * 2, 0x00020000);
      PIN(wr); PIN(wr0); PIN(rd); PIN(sv); PIN(gv); SPIN(dst); SPIN_ALL(so, 7);
      HINT_PREP(4, w == 3 && layer == 0);
      f32x2 r = {0.f, 0.f};
      if (w == 1 || w == 2) {
        hint_wait<D_R, GSLEEP>(rs, lg(LG_HINT) + sd + (layer * 8 + (blk & 7)) * 128);
        const u32x2 pr = gather_pair(rs, ro, rb);
        r = f32x2{bflo(pr[0]) + bflo(pr[1]) + bflo(bia), bfhi(pr[0]) + bfhi(pr[1]) + bfhi(bia)};
      }
      PIN(r);
      HINT_GET(layer, 2 + layer, D_H);
      f32x4 acc[2];
      gather_use<7>(rs, gv, so, [&](auto I, const u32x4& v) {
        constexpr int s = I.value;
        if constexpr (s == 6) HINT_PUT();
        MFMA_VV(s == 0, acc[0], wf[2 * s], v); MFMA_VV(s == 0, acc[1], wf[2 * s + 1], v);
      });
      asm volatile("s_nop 6" : "+v"(acc[0]), "+v"(acc[1]));
      *(LAS f32x4*)(L + wr0) = acc[0];
      asm volatile("" ::: "memory");
      *(LAS f32x4*)(L + wr + 256) = acc[1];
      BAR();
      f32x2 p[8];
#pragma unroll
      for (int i = 0; i < 8; ++i) p[i] = *(LAS const f32x2*)(L + rd + i * 512);
      r = (((r + p[0]) + p[1]) + (p[2] + p[3])) + ((p[4] + p[5]) + (p[6] + p[7]));
      __builtin_amdgcn_raw_buffer_store_b32(nosent(pack_bf16(r[0], r[1])), rdst, sv, dst, 16);
    } else if (role == R_WQ && layer == 0) {
      int so[7];
#pragma unroll
      for (int i = 0; i < 7; ++i) so[i] = i == 6 && q3 == 2 ? OOB : lg(LG_ONE + (q3 * 7 + i) * 512) + sd;
      const int cs_ = ((qh << 11) ^ pol) & 8192;
      int vo = (lane >> 5) * 16384 + w * 128 + ((lane >> 4) & 1) * 64 + ((lane >> 1) & 1) * 32 + ((lane >> 2) & 3) * 8 + (lane & 1) * 4, dst = lg(LG_Q1 + ((qh >> 3) * 4 + (qh & 3)) * 16384) + cs_ + q3 * 512;
      int sp = n < 8 && (g == 2 || g == (n >> 2)) ? n * 8 + (g == 2 ? 0 : 4) : OOB, sdst = w == 3 ? lg(LG_SP + (qh * 4 + q3) * 128) + cs_ : OOB;
      int gv = n < 8 ? 2 * (LG_XM - LG_ONE) + g * 128 + n * 16 : 0;
      PIN(vo); PIN(sp); PIN(gv); SPIN(dst); SPIN(sdst); SPIN_ALL(so, 7);
      HINT_PREP(5, w == 1 && q3 == 0);
      HINT_GET(2, 4, 0);
      f32x4 acc[2], sq;
      gather_use<7>(rs, gv, so, [&](auto I, const u32x4& v) {
        constexpr int s = I.value;
        if constexpr (s == 6) HINT_PUT();
        MFMA_VV(s == 0, sq, v, v); MFMA_VV(s == 0, acc[0], v, wf[2 * s]); MFMA_VV(s == 0, acc[1], v, wf[2 * s + 1]);
      });
      float q, tmp;
      asm volatile("s_nop 5\n\tv_cndmask_b32 %0, %2, %3, %6\n\tv_cndmask_b32 %1, %4, %5, %6\n\tv_cndmask_b32 %0, %0, %1, %7"
                   : "=&v"(q), "=&v"(tmp) : "v"(sq[0]), "v"(sq[1]), "v"(sq[2]), "v"(sq[3]), "s"(0x00000000AAAAAAAAull), "s"(0x00000000CCCCCCCCull));
      wq_publish<false>(rs, acc, vo, dst);
      __builtin_amdgcn_raw_buffer_store_b32(nosent(__float_as_uint(q)), rs, sp, sdst, 16);
    }
    if (layer == 1) break;
    {
      const int lane = lane_id(), g = lane >> 4, n = lane & 15;
      LAS char* vl = L + w * (NVL * 1024) + lane * 16;
      u32x4 qb[4] = {};
      u32x8 ring[4];
      f32x2 qcs = *(LAS const f32x2*)(L + L_QCS + lane * 8) * (-1.0f / DM);
      int wr = L_MRG + w * 512 + (8 * n + 4 * (g & 1) + (g & 2)) * 4, rd = L_MRG + lane * 8, ix = n < 8 ? 0x4444 : 0xEEEE;
      int qv = lane * 8, qv2 = (n & 3) < 3 ? (n & 3) * 128 : OOB;
      int dst = copy_of(w, lg(LG_O + 65536 + odst), sd) - L_MRG / 2;
      int qs0 = lg(LG_Q1 + ((hh >> 3) * 4 + (hh & 3)) * 16384 + (bb >> 2) * 8192) + sd + (bb & 3) * 1536, qs2 = lg(LG_SP + hh * 512) + sd + bb * 8;
      PIN(qcs); PIN(qb[0]); PIN(qb[1]); PIN(qb[2]); PIN(qb[3]); PIN(wr); PIN(rd); PIN(ix); PIN(qv); PIN(qv2); SPIN(dst); SPIN(qs0); SPIN(qs2);
#pragma unroll
      for (int t = 0; t < 4; ++t) ring[t] = lds_pair(vl + t * 2048);
      if (role != R_WQ) HINT_GET(4, 5, 0);
      f32x2 q2, st2;
      gather_q(rs, qv, qv2, qs0, qs2, q2, st2);
      q2 += st2[0] * qcs;
      float s[4], c;
      qk(kf, pack_bf16(q2[0], q2[1]), qb, s, [&](auto G) {
        if constexpr (G.value == 0) { PIN(st2); st2 *= 1.0f / DM; c = fmaxf(fmaf(-st2[0], st2[0], st2[1]), 0.f) + LN_EPS; PIN(c); }
        if constexpr (G.value == 1) { c = SC_L2E * __builtin_amdgcn_rsqf(c); PIN(c); }
      });
      const Soft sm = softmax(s, c);
      f32x4 o[8];
      float fac, ls;
      f32x2 ml;
      u32 zl = sm.p01, zh = sm.p23;
      asm volatile("s_nop 1\n\tv_mov_b32_dpp %0, %3 row_ror:8 row_mask:0xf bank_mask:0xc\n\tv_mov_b32_dpp %1, %2 row_ror:8 row_mask:0xf bank_mask:0x3\n\t"
                   "s_nop 1\n\tv_permlane32_swap_b32 %0, %1\n\ts_nop 1\n\tv_permlane16_swap_b32 %0, %1\n\ts_nop 0"
                   : "+&v"(zl), "+&v"(zh) : "v"(sm.p01), "v"(sm.p23));
      u32x4 a0 = sp_a<0>(zl, zh), a1;
      PIN(a0);
      zero_o<0>(o[0]); zero_o<4>(o[4]);
      asm volatile("s_nop 0");
      auto step = [&](auto FF_) {
        constexpr int f = FF_.value, t = pv_t(f), P = pv_p(f), k = pv_k(f), nx = pv_slot(k + 4);
        if constexpr (v_in_lds(P, t)) {
          SMFMA(o[t], (f & 1) ? a1 : a0, ring[k & 3], ix);
          if constexpr (k + 4 < 19) ring[k & 3] = lds_pair(vl + nx * 1024);
        } else SMFMA(o[t], a1, vp[v_reg(P, t)], ix);
        if constexpr (f == 0) { a1 = sp_a<3>(zl, zh); PIN(a1); zero_o<1>(o[1]); zero_o<5>(o[5]); }
        if constexpr (f >= 1 && f < 5) zero_o<pv_t(f + 3)>(o[pv_t(f + 3)]);
        if constexpr (f == 14) { PIN(zl); PIN(zh); a0 = sp_a<1>(zl, zh); PIN(a0); }
        if constexpr (f == 15) { PIN(zl); PIN(zh); a1 = sp_a<2>(zl, zh); PIN(a1); }
        if constexpr (f == 1) { ls = dot2(sm.p01, ONES, dot2(sm.p23, ONES, 0.f)); PIN(ls); }
        if constexpr (f == 3) asm volatile("v_add_f32_dpp %0, %0, %0 quad_perm:[1,0,3,2] row_mask:0xf bank_mask:0xf" : "+v"(ls));
        if constexpr (f == 4) asm volatile("v_add_f32_dpp %0, %0, %0 quad_perm:[2,3,0,1] row_mask:0xf bank_mask:0xf" : "+v"(ls));
        if constexpr (f == 5) asm volatile("v_add_f32_dpp %0, %0, %0 row_ror:4 row_mask:0xf bank_mask:0xf" : "+v"(ls));
        if constexpr (f == 6) asm volatile("v_add_f32_dpp %0, %0, %0 row_ror:8 row_mask:0xf bank_mask:0xf" : "+v"(ls));
        if constexpr (f == 7) asm volatile("v_add_f32_dpp %0, %0, %0 row_bcast:15 row_mask:0xa bank_mask:0xf" : "+v"(ls));
        if constexpr (f == 8) asm volatile("v_add_f32_dpp %0, %0, %0 row_bcast:31 row_mask:0xc bank_mask:0xf" : "+v"(ls));
        if constexpr (f == 9) *(LAS f32x2*)(L + L_ML + w * 8) = f32x2{sm.m, rl63(ls)};
        if constexpr (f == 19) {
          asm volatile("s_barrier" ::: "memory");
          ml = *(LAS const f32x2*)(L + L_ML + ((wr >> 2) & 24));
          asm volatile("" ::: "memory");
        }
        if constexpr (f == 25) asm volatile("v_max_f32_dpp %0, %1, %1 quad_perm:[1,0,3,2] row_mask:0xf bank_mask:0xf" : "=v"(fac) : "v"(ml[0]));
        if constexpr (f == 26) asm volatile("s_nop 0\n\tv_max_f32_dpp %0, %0, %0 quad_perm:[2,3,0,1] row_mask:0xf bank_mask:0xf" : "+v"(fac));
        if constexpr (f == 27) { ls = __builtin_amdgcn_exp2f(ml[0] - fac); fac = sm.m - fac; PIN(ls); PIN(fac); }
        if constexpr (f == 28) { ls *= ml[1]; fac = __builtin_amdgcn_exp2f(fac); PIN(ls); PIN(fac); }
        if constexpr (f == 29) asm volatile("s_nop 0\n\tv_add_f32_dpp %0, %0, %0 quad_perm:[1,0,3,2] row_mask:0xf bank_mask:0xf" : "+v"(ls));
        if constexpr (f == 30) asm volatile("s_nop 0\n\tv_add_f32_dpp %0, %0, %0 quad_perm:[2,3,0,1] row_mask:0xf bank_mask:0xf" : "+v"(ls));
      };
      for_seq(step, std::make_integer_sequence<int, 32>{});
      {
        float y[8];
#pragma unroll
        for (int t = 0; t < 8; ++t) y[t] = o[t][0];
        asm volatile("v_rcp_f32 %9, %9\n\ts_nop 0\n\tv_mul_f32 %8, %8, %9\n\t"
                     "v_permlane16_swap_b32 %0, %4\n\tv_permlane16_swap_b32 %1, %5\n\ts_nop 0\n\tv_permlane16_swap_b32 %2, %6\n\ts_nop 0\n\tv_permlane16_swap_b32 %3, %7\n\t"
                     "v_permlane32_swap_b32 %0, %2\n\ts_nop 0\n\tv_permlane32_swap_b32 %1, %3"
                     : "+v"(y[0]), "+v"(y[1]), "+v"(y[2]), "+v"(y[3]), "+v"(y[4]), "+v"(y[5]), "+v"(y[6]), "+v"(y[7]), "+v"(fac), "+v"(ls));
        sum_publish(L, rs, wr, rd, f32x2{(y[0] + y[2]) * fac, (y[1] + y[3]) * fac}, dst);
      }
    }
  }
  asm volatile(".Lfk_end:" ::: "memory");
}

}

extern "C" void kernel_launch(void* const* d_in, const int* in_sizes, int n_in, void* d_out, int out_size, void* d_ws, size_t ws_size,
                              hipStream_t stream) {
  Args a;
  a.wp[R_WO] = (const char*)d_in[4]; a.wp[R_W2] = (const char*)d_in[8]; a.wp[R_WQ] = (const char*)d_in[3]; a.wp[R_W1] = (const char*)d_in[6];
  a.ws = (char*)d_ws; a.kc = (const char*)d_in[1]; a.vc = (const char*)d_in[2]; a.x = (const u16*)d_in[0];
  a.b1 = (const u16*)d_in[7]; a.b2 = (const u16*)d_in[9]; a.out = (u16*)d_out;
  (void)hipFuncSetAttribute((const void*)fused_kernel, hipFuncAttributeMaxDynamicSharedMemorySize, LDS_BYTES);
  hipLaunchKernelGGL(fused_kernel, dim3(256), dim3(256), LDS_BYTES, stream, a);
}
```
